```python
import torch

def _softmax(x, axis=-1):
    return torch.softmax(x, dim=axis)

def _gelu(x):
    return torch.nn.functional.gelu(x, approximate='tanh')

def _silu(x):
    return torch.nn.functional.silu(x)

def _sigmoid(x):
    return torch.sigmoid(x)

def _layernorm(x, axis=-1, eps=1e-05):
    m = x.mean(dim=axis, keepdim=True)
    v = x.var(dim=axis, keepdim=True, unbiased=False)
    return (x - m) / torch.sqrt(v + eps)

def _scatter_add(tgt, idx, src, axis=0):
    return tgt.clone().index_add_(axis, idx, src)

def _segment_sum(src, seg, n):
    out = torch.zeros((n,) + src.shape[1:], dtype=src.dtype)
    return out.index_add_(0, seg, src)

def _pad(x, pw):
    flat = tuple((v for pair in reversed(pw) for v in pair))
    return torch.nn.functional.pad(x, flat)
kernel_name = 'adaLN_cond__spectral_fnet__moe_arch_mod__prompt'

def setup_inputs():

    _w = lambda a: (lambda t: t.to(torch.bfloat16) if t.is_floating_point() else t)(torch.as_tensor(a))

    def _A_setup():
        B, S, D = (1, 2048, 512)
        ns = torch.arange(S)[:, None] * torch.arange(S)[None, :]
        nd = torch.arange(D)[:, None] * torch.arange(D)[None, :]
        Ws_re = torch.cos(-2.0 * torch.pi * ns / S).to(float)
        Ws_im = torch.sin(-2.0 * torch.pi * ns / S).to(float)
        Wd_re = torch.cos(-2.0 * torch.pi * nd / D).to(float)
        Wd_im = torch.sin(-2.0 * torch.pi * nd / D).to(float)
        return {'x': _w((lambda *s: torch.randn(*s, dtype=torch.bfloat16) * 0.05)(B, S, D)), 'dft_seq_re': _w(Ws_re), 'dft_seq_im': _w(Ws_im), 'dft_hid_re': _w(Wd_re), 'dft_hid_im': _w(Wd_im)}
    _A_in = _A_setup()

    def _B_setup():
        B, S, D, F, C = (1, 2048, 512, 1792, 2)
        return {'x': _w((lambda *s: torch.randn(*s, dtype=torch.bfloat16) * 0.05)(B, S, D)), 'w_r': _w((lambda *s: torch.randn(*s, dtype=torch.bfloat16) * 3.2)(D)), 'w_gate': _w((lambda *s: torch.randn(*s, dtype=torch.bfloat16) * 0.8)(D, F)), 'w_up': _w((lambda *s: torch.randn(*s, dtype=torch.bfloat16) * 0.8)(D, F)), 'w_down': _w((lambda *s: torch.randn(*s, dtype=torch.bfloat16) * 0.8)(F, D))}
    _B_in = _B_setup()
    return {'A_x': _A_in['x'], 'A_dft_seq_re': _A_in['dft_seq_re'], 'A_dft_seq_im': _A_in['dft_seq_im'], 'A_dft_hid_re': _A_in['dft_hid_re'], 'A_dft_hid_im': _A_in['dft_hid_im'], 'B_x': _B_in['x'], 'B_w_r': _B_in['w_r'], 'B_w_gate': _B_in['w_gate'], 'B_w_up': _B_in['w_up'], 'B_w_down': _B_in['w_down'], 'c': _w((lambda *s: torch.randn(*s, dtype=torch.bfloat16) * 0.1)(1, 512) * 8.0), 'w_mod': _w((lambda *s: torch.randn(*s, dtype=torch.bfloat16) * 0.1)(512, 6 * 512) * 512 ** (-0.5) * 8.0)}

def reference(A_x, A_dft_seq_re, A_dft_seq_im, A_dft_hid_re, A_dft_hid_im, B_x, B_w_r, B_w_gate, B_w_up, B_w_down, c, w_mod):

    def _A_ref(x, dft_seq_re, dft_seq_im, dft_hid_re, dft_hid_im):
        a = torch.einsum('bsd,ed->bse', x, dft_hid_re)
        b = torch.einsum('bsd,ed->bse', x, dft_hid_im)
        y = torch.einsum('ts,bse->bte', dft_seq_re, a) - torch.einsum('ts,bse->bte', dft_seq_im, b)
        return y

    def _B_ref(x, w_r, w_gate, w_up, w_down):
        B, S, D = x.shape
        C = 2
        r = x @ w_r
        idx = torch.argsort(-r, axis=-1)[:, :C]
        idx_e = (lambda x, n, axis: torch.repeat_interleave(x, n, dim=axis))(idx.reshape(B, C, 1), D, axis=2)
        x_sel = (lambda x, i, axis: torch.take_along_dim(x, i, dim=axis))(x, idx_e, axis=1)
        g = _silu(x_sel @ w_gate)
        u = x_sel @ w_up
        y = g * u @ w_down
        r_sel = _sigmoid((lambda x, i, axis: torch.take_along_dim(x, i, dim=axis))(r, idx, axis=1))
        y = y * r_sel.reshape(B, C, 1)
        b_off = (torch.arange(B) * S).reshape(B, 1)
        flat_idx = (idx + b_off).reshape(B * C)
        zero = (x * 0.0).reshape(B * S, D)
        upd = _scatter_add(zero, flat_idx, y.reshape(B * C, D), axis=0)
        return x + upd.reshape(B, S, D)
    x = A_x
    mod = (_silu(c) @ w_mod).reshape(c.shape[0], 6, 1, -1)
    sa, ca, ga, sb, cb, gb = (mod[:, 0].clone(), mod[:, 1].clone(), mod[:, 2].clone(), mod[:, 3].clone(), mod[:, 4].clone(), mod[:, 5].clone())
    h = _layernorm(x, axis=-1) * (1 + ca) + sa
    x = x + ga * _A_ref(h, A_dft_seq_re, A_dft_seq_im, A_dft_hid_re, A_dft_hid_im)
    h = _layernorm(x, axis=-1) * (1 + cb) + sb
    x = x + gb * _B_ref(h, B_w_r, B_w_gate, B_w_up, B_w_down)
    return x
```

```cpp
#include <hip/hip_runtime.h>
#include <hip/hip_bf16.h>
typedef unsigned short u16;
typedef __attribute__((ext_vector_type(8))) short bf16x8;
typedef __attribute__((ext_vector_type(16))) float f32x16;
typedef __attribute__((ext_vector_type(4))) float f32x4;
typedef __attribute__((ext_vector_type(2))) unsigned u32x2;
typedef __attribute__((ext_vector_type(4))) unsigned u32x4;
#define LAS __attribute__((address_space(3)))
#define GAS __attribute__((address_space(1)))

constexpr int S = 2048, D = 512, F = 1792;

__device__ __forceinline__ unsigned pk2(float a, float b) {
  unsigned r; asm("v_cvt_pk_bf16_f32 %0, %1, %2" : "=v"(r) : "v"(a), "v"(b)); return r;
}
__device__ __forceinline__ float bfr(float a) { return __uint_as_float(pk2(a, a) << 16); }
__device__ __forceinline__ float lo16(unsigned u) { return __uint_as_float(u << 16); }
__device__ __forceinline__ float hi16(unsigned u) { return __uint_as_float(u & 0xffff0000u); }
__device__ __forceinline__ float bf2f(u16 u) { return __uint_as_float((unsigned)u << 16); }
template<int N> __device__ __forceinline__ void wait_vm() { asm volatile("s_waitcnt vmcnt(%0)" :: "n"(N) : "memory"); }
__device__ __forceinline__ float silu(float x) { return x / (1.f + __expf(-x)); }
__device__ __forceinline__ float wave_sum(float v) {
#pragma unroll
  for (int o = 32; o; o >>= 1) v += __shfl_xor(v, o);
  return v;
}

constexpr size_t WS_N8 = 0;
constexpr size_t WS_PQ = 2u << 20;
constexpr size_t WS_X1 = 6u << 20;
constexpr size_t WS_MOD = 8u << 20;
constexpr size_t WS_R = WS_MOD + 65536;
constexpr size_t WS_H2 = 9u << 20;
constexpr size_t WS_ACT = 11u << 20;
constexpr size_t WS_SEL = WS_ACT + 65536;

__device__ __forceinline__ float modv(const float* modp, int j) {
  return bfr((modp[j] + modp[3072 + j]) + (modp[2 * 3072 + j] + modp[3 * 3072 + j]));
}

__global__ __launch_bounds__(256) void k1(const u16* __restrict__ x, const u16* __restrict__ c, const u16* __restrict__ wmod,
                                          u16* __restrict__ n8, float* __restrict__ modp) {
  __shared__ float red[4][64];
  const int tid = threadIdx.x, l = tid & 63, w = tid >> 6;
  if (blockIdx.x < 256) {
    const int b = blockIdx.x;
    unsigned v[8];
#pragma unroll
    for (int r = 0; r < 8; ++r) v[r] = *(const unsigned*)(x + (size_t)(8 * b + r) * D + 2 * tid);
    float mean[8], rs[8];
#pragma unroll
    for (int r = 0; r < 8; ++r) { float s = wave_sum(lo16(v[r]) + hi16(v[r])); if (l == 0) red[w][r] = s; }
    __syncthreads();
#pragma unroll
    for (int r = 0; r < 8; ++r) mean[r] = (red[0][r] + red[1][r] + red[2][r] + red[3][r]) * (1.f / D);
#pragma unroll
    for (int r = 0; r < 8; ++r) { float a = lo16(v[r]) - mean[r], bb = hi16(v[r]) - mean[r]; float s = wave_sum(a * a + bb * bb); if (l == 0) red[w][8 + r] = s; }
    __syncthreads();
#pragma unroll
    for (int r = 0; r < 8; ++r) rs[r] = rsqrtf((red[0][8 + r] + red[1][8 + r] + red[2][8 + r] + red[3][8 + r]) * (1.f / D) + 1e-5f);
    u32x4 o0, o1;
#pragma unroll
    for (int q = 0; q < 4; ++q) {
      o0[q] = pk2((lo16(v[2 * q]) - mean[2 * q]) * rs[2 * q], (lo16(v[2 * q + 1]) - mean[2 * q + 1]) * rs[2 * q + 1]);
      o1[q] = pk2((hi16(v[2 * q]) - mean[2 * q]) * rs[2 * q], (hi16(v[2 * q + 1]) - mean[2 * q + 1]) * rs[2 * q + 1]);
    }
    u32x4* dst = (u32x4*)(n8 + ((size_t)b * D + 2 * tid) * 8);
    dst[0] = o0; dst[1] = o1;
  } else {
    const int b = blockIdx.x - 256;
    const int jc = b % 48, kc = b / 48;
    const int rg = tid >> 3, cc = tid & 7;
    float acc[8] = {};
    u32x4 wv[4]; float sc[4];
#pragma unroll
    for (int i = 0; i < 4; ++i) {
      int row = kc * 128 + rg + 32 * i;
      wv[i] = *(const u32x4*)(wmod + (size_t)row * 3072 + jc * 64 + cc * 8);
      sc[i] = bfr(silu(bf2f(c[row])));
    }
#pragma unroll
    for (int i = 0; i < 4; ++i)
#pragma unroll
      for (int q = 0; q < 4; ++q) { acc[2 * q] += sc[i] * lo16(wv[i][q]); acc[2 * q + 1] += sc[i] * hi16(wv[i][q]); }
#pragma unroll
    for (int q = 0; q < 8; ++q) { acc[q] += __shfl_xor(acc[q], 8); acc[q] += __shfl_xor(acc[q], 16); acc[q] += __shfl_xor(acc[q], 32); }
    if (l < 8) {
#pragma unroll
      for (int q = 0; q < 8; ++q) red[w][l * 8 + q] = acc[q];
    }
    __syncthreads();
    if (tid < 64) modp[kc * 3072 + jc * 64 + tid] = red[0][tid] + red[1][tid] + red[2][tid] + red[3][tid];
  }
}

constexpr int NST = 4;
__global__ __launch_bounds__(256, 1) void k2(const u16* __restrict__ Wre, const u16* __restrict__ Wim, const u16* __restrict__ n8,
                                             const float* __restrict__ modp, u16* __restrict__ PQ) {
  extern __shared__ __attribute__((aligned(16))) char smem[];
  const int tid = threadIdx.x, l = tid & 63;
  const int w = __builtin_amdgcn_readfirstlane(tid >> 6);
  const int g = blockIdx.x & 7, idx = blockIdx.x >> 3;
  const int p = 4 * g + (idx >> 3), j = idx & 7;
  const u16* Wsrc = (w < 2 ? Wre : Wim) + (size_t)(64 * p + 32 * (w & 1)) * 2048;
  const int lr = l >> 3;
  const u16* asrc[4];
#pragma unroll
  for (int i = 0; i < 4; ++i) { int row = 8 * i + lr; int ch = (l & 7) ^ ((row >> 1) & 7); asrc[i] = Wsrc + (size_t)row * 2048 + ch * 8; }
  const u16* bsrc = n8 + ((size_t)(2 * w) * 512 + 64 * j + l) * 8;
  constexpr int STB = 24576;
  auto stage = [&](int kt) {
    char* sb = smem + (kt % NST) * STB;
#pragma unroll
    for (int i = 0; i < 4; ++i)
      __builtin_amdgcn_global_load_lds((const GAS void*)(asrc[i] + kt * 64), (LAS void*)(sb + w * 4096 + i * 1024), 16, 0, 0);
#pragma unroll
    for (int i = 0; i < 2; ++i)
      __builtin_amdgcn_global_load_lds((const GAS void*)(bsrc + ((size_t)kt * 8 + i) * 4096), (LAS void*)(sb + 16384 + (2 * w + i) * 1024), 16, 0, 0);
  };
  f32x16 acc[2] = {};
  float rsum = 0.f;
  const int h = l >> 5, r = l & 31;
  const int aoff = w * 4096 + r * 128;
  const int sw = (r >> 1) & 7;
  const int boff = 16384 + h * 1024 + r * 16;
#pragma unroll
  for (int s = 0; s < NST - 1; ++s) stage(s);
#pragma unroll 1
  for (int kt = 0; kt < 32; ++kt) {
    int rem = 31 - kt;
    if (rem >= NST - 2) wait_vm<6 * (NST - 2)>();
    else if (NST > 3 && rem == NST - 3) wait_vm<6 * (NST > 3 ? NST - 3 : 0)>();
    else wait_vm<0>();
    __builtin_amdgcn_s_barrier();
    if (kt + NST - 1 < 32) stage(kt + NST - 1);
    const char* sb = smem + (kt % NST) * STB;
    bf16x8 af[4], bf[4][2];
#pragma unroll
    for (int kk = 0; kk < 4; ++kk) {
      af[kk] = *(const bf16x8*)(sb + aoff + (((2 * kk + h) ^ sw) << 4));
      bf[kk][0] = *(const bf16x8*)(sb + boff + kk * 2048);
      bf[kk][1] = *(const bf16x8*)(sb + boff + kk * 2048 + 512);
    }
#pragma unroll
    for (int kk = 0; kk < 4; ++kk) {
      acc[0] = __builtin_amdgcn_mfma_f32_32x32x16_bf16(bf[kk][0], af[kk], acc[0], 0, 0, 0);
      acc[1] = __builtin_amdgcn_mfma_f32_32x32x16_bf16(bf[kk][1], af[kk], acc[1], 0, 0, 0);
      u32x4 u = __builtin_bit_cast(u32x4, af[kk]);
#pragma unroll
      for (int q = 0; q < 4; ++q) rsum += lo16(u[q]) + hi16(u[q]);
    }
  }
  rsum += __shfl_xor(rsum, 32);
  const float sgn = w < 2 ? 1.f : -1.f;
#pragma unroll
  for (int db = 0; db < 2; ++db)
#pragma unroll
    for (int q = 0; q < 4; ++q) {
      int kg = (w >> 1) * 64 + 8 * j + 4 * db + q;
      float o[4];
#pragma unroll
      for (int e = 0; e < 4; ++e) {
        int d = 64 * j + 32 * db + 8 * q + 4 * h + e;
        float sa = modv(modp, d), ca1 = bfr(1.f + modv(modp, 512 + d));
        o[e] = sgn * (acc[db][4 * q + e] * ca1 + rsum * sa);
      }
      u32x2 v; v.x = pk2(o[0], o[1]); v.y = pk2(o[2], o[3]);
      *(u32x2*)(PQ + (((size_t)p * 128 + kg) * 64 + 32 * (w & 1) + r) * 8 + 4 * h) = v;
    }
}

__global__ __launch_bounds__(256, 1) void k3(const u16* __restrict__ PQ, const u16* __restrict__ Dre, const u16* __restrict__ Dim,
                                             const u16* __restrict__ x, const float* __restrict__ modp, u16* __restrict__ x1) {
  extern __shared__ __attribute__((aligned(16))) char smem[];
  const int tid = threadIdx.x, l = tid & 63;
  const int w = __builtin_amdgcn_readfirstlane(tid >> 6);
  const int g = blockIdx.x & 7, idx = blockIdx.x >> 3;
  const int p = 4 * g + (idx >> 3), j = idx & 7;
  constexpr int NS3 = 8, STB = 16384;
  const int lr = l >> 3;
  int aoffs[2];
#pragma unroll
  for (int i = 0; i < 2; ++i) { int row = 16 * w + 8 * i + lr; int ch = (l & 7) ^ ((row >> 1) & 7); aoffs[i] = (64 * j + row) * 512 + ch * 8; }
  const u16* psrc = PQ + ((size_t)p * 128 + 2 * w) * 512 + l * 8;
  auto stage = [&](int kt) {
    char* sb = smem + (kt % NS3) * STB;
    const u16* Wd = (kt < 8 ? Dre : Dim) + (kt & 7) * 64;
#pragma unroll
    for (int i = 0; i < 2; ++i)
      __builtin_amdgcn_global_load_lds((const GAS void*)(Wd + aoffs[i]), (LAS void*)(sb + (2 * w + i) * 1024), 16, 0, 0);
#pragma unroll
    for (int i = 0; i < 2; ++i)
      __builtin_amdgcn_global_load_lds((const GAS void*)(psrc + ((size_t)kt * 8 + i) * 512), (LAS void*)(sb + 8192 + (2 * w + i) * 1024), 16, 0, 0);
  };
  f32x16 acc = {};
  const int h = l >> 5, r = l & 31;
  const int we = w & 1, wt = w >> 1;
  const int woff = (32 * we + r) * 128, sw = (r >> 1) & 7;
  const int poff = 8192 + h * 1024 + (32 * wt + r) * 16;
#pragma unroll
  for (int s = 0; s < NS3 - 1; ++s) stage(s);
#pragma unroll
  for (int kt = 0; kt < 16; ++kt) {
    int rem = 15 - kt;
    if (rem >= 6) wait_vm<24>(); else if (rem == 5) wait_vm<20>(); else if (rem == 4) wait_vm<16>(); else if (rem == 3) wait_vm<12>();
    else if (rem == 2) wait_vm<8>(); else if (rem == 1) wait_vm<4>(); else wait_vm<0>();
    __builtin_amdgcn_s_barrier();
    if (kt + NS3 - 1 < 16) stage(kt + NS3 - 1);
    const char* sb = smem + (kt % NS3) * STB;
#pragma unroll
    for (int kk = 0; kk < 4; ++kk) {
      bf16x8 wf = *(const bf16x8*)(sb + woff + (((2 * kk + h) ^ sw) << 4));
      bf16x8 pf = *(const bf16x8*)(sb + poff + kk * 2048);
      acc = __builtin_amdgcn_mfma_f32_32x32x16_bf16(wf, pf, acc, 0, 0, 0);
    }
  }
  const int t = 64 * p + 32 * wt + r;
#pragma unroll
  for (int q = 0; q < 4; ++q) {
    int e0 = 64 * j + 32 * we + 8 * q + 4 * h;
    u32x2 xv = *(const u32x2*)(x + (size_t)t * D + e0);
    float xs[4] = {lo16(xv.x), hi16(xv.x), lo16(xv.y), hi16(xv.y)};
    float o[4];
#pragma unroll
    for (int e = 0; e < 4; ++e) o[e] = xs[e] + bfr(modv(modp, 1024 + e0 + e) * bfr(acc[4 * q + e]));
    u32x2 v; v.x = pk2(o[0], o[1]); v.y = pk2(o[2], o[3]);
    *(u32x2*)(x1 + (size_t)t * D + e0) = v;
  }
}

__global__ __launch_bounds__(256) void k4(const u16* __restrict__ x1, const float* __restrict__ modp, const u16* __restrict__ wr,
                                          u16* __restrict__ h2, float* __restrict__ rr, u16* __restrict__ out) {
  const int tid = threadIdx.x, l = tid & 63, w = tid >> 6;
  const int t = blockIdx.x * 4 + w;
  u32x4 xv = *(const u32x4*)(x1 + (size_t)t * D + l * 8);
  u32x4 wv = *(const u32x4*)(wr + l * 8);
  float xs[8], ws_[8];
#pragma unroll
  for (int q = 0; q < 4; ++q) { xs[2 * q] = lo16(xv[q]); xs[2 * q + 1] = hi16(xv[q]); ws_[2 * q] = lo16(wv[q]); ws_[2 * q + 1] = hi16(wv[q]); }
  float s = 0;
#pragma unroll
  for (int e = 0; e < 8; ++e) s += xs[e];
  float mean = wave_sum(s) * (1.f / D);
  float v = 0;
#pragma unroll
  for (int e = 0; e < 8; ++e) v += (xs[e] - mean) * (xs[e] - mean);
  float rs = rsqrtf(wave_sum(v) * (1.f / D) + 1e-5f);
  float hh[8], oo[8], racc = 0;
#pragma unroll
  for (int e = 0; e < 8; ++e) {
    int col = l * 8 + e;
    float sb = modv(modp, 3 * 512 + col), cb1 = bfr(1.f + modv(modp, 4 * 512 + col)), gb = modv(modp, 5 * 512 + col);
    hh[e] = bfr((xs[e] - mean) * rs * cb1 + sb);
    racc += hh[e] * ws_[e];
    oo[e] = xs[e] + bfr(gb * hh[e]);
  }
  racc = wave_sum(racc);
  if (l == 0) rr[t] = racc;
  u32x4 ho, o;
#pragma unroll
  for (int q = 0; q < 4; ++q) { ho[q] = pk2(hh[2 * q], hh[2 * q + 1]); o[q] = pk2(oo[2 * q], oo[2 * q + 1]); }
  *(u32x4*)(h2 + (size_t)t * D + l * 8) = ho;
  *(u32x4*)(out + (size_t)t * D + l * 8) = o;
}

__global__ __launch_bounds__(256) void k5(const float* __restrict__ rr, const u16* __restrict__ h2, const u16* __restrict__ wg,
                                          const u16* __restrict__ wu, float* __restrict__ act, float* __restrict__ sel) {
  __shared__ unsigned long long sk[8]; __shared__ float hs[2][512]; __shared__ float red[4][4][32];
  const int tid = threadIdx.x, l = tid & 63, w = tid >> 6;
  const int rg = tid >> 2, cc = tid & 3, f0 = blockIdx.x * 32;
  u32x4 gv[8], uv[8];
#pragma unroll
  for (int i = 0; i < 8; ++i) {
    gv[i] = *(const u32x4*)(wg + (size_t)(rg + 64 * i) * F + f0 + cc * 8);
    uv[i] = *(const u32x4*)(wu + (size_t)(rg + 64 * i) * F + f0 + cc * 8);
  }
  unsigned long long k1_ = 0, k2_ = 0;
#define INS(kk) { unsigned long long k_ = (kk); unsigned long long lo_ = k_ < k1_ ? k_ : k1_; k1_ = k_ < k1_ ? k1_ : k_; k2_ = lo_ > k2_ ? lo_ : k2_; }
#pragma unroll
  for (int i = 0; i < 8; ++i) { unsigned u = __float_as_uint(bfr(rr[tid + 256 * i])); u = (u & 0x80000000u) ? ~u : (u | 0x80000000u);
    INS(((unsigned long long)u << 32) | (unsigned)(0xffffffffu - (tid + 256 * i))); }
#pragma unroll
  for (int o = 32; o; o >>= 1) {
    unsigned long long a1 = __shfl_xor(k1_, o), a2 = __shfl_xor(k2_, o);
    INS(a1); INS(a2);
  }
  if (l == 0) { sk[2 * w] = k1_; sk[2 * w + 1] = k2_; }
  __syncthreads();
  k1_ = k2_ = 0;
#pragma unroll
  for (int i = 0; i < 8; ++i) INS(sk[i]);
  const int i1 = 0xffffffffu - (unsigned)k1_, i2 = 0xffffffffu - (unsigned)k2_;
  unsigned u1 = k1_ >> 32, u2 = k2_ >> 32;
  const float v1 = __uint_as_float((u1 & 0x80000000u) ? (u1 & 0x7fffffffu) : ~u1), v2 = __uint_as_float((u2 & 0x80000000u) ? (u2 & 0x7fffffffu) : ~u2);
  if (blockIdx.x == 0 && tid == 0) { ((int*)sel)[0] = i1; ((int*)sel)[1] = i2; sel[2] = v1; sel[3] = v2; }
  { unsigned a = *(const unsigned*)(h2 + (size_t)i1 * D + 2 * tid), b = *(const unsigned*)(h2 + (size_t)i2 * D + 2 * tid);
    hs[0][2 * tid] = lo16(a); hs[0][2 * tid + 1] = hi16(a); hs[1][2 * tid] = lo16(b); hs[1][2 * tid + 1] = hi16(b); }
  __syncthreads();
  float ag[2][8] = {}, au[2][8] = {};
#pragma unroll
  for (int i = 0; i < 8; ++i) {
    float h0 = hs[0][rg + 64 * i], h1 = hs[1][rg + 64 * i];
#pragma unroll
    for (int q = 0; q < 4; ++q) {
      ag[0][2 * q] += h0 * lo16(gv[i][q]); ag[0][2 * q + 1] += h0 * hi16(gv[i][q]);
      ag[1][2 * q] += h1 * lo16(gv[i][q]); ag[1][2 * q + 1] += h1 * hi16(gv[i][q]);
      au[0][2 * q] += h0 * lo16(uv[i][q]); au[0][2 * q + 1] += h0 * hi16(uv[i][q]);
      au[1][2 * q] += h1 * lo16(uv[i][q]); au[1][2 * q + 1] += h1 * hi16(uv[i][q]);
    }
  }
#pragma unroll
  for (int m = 0; m < 2; ++m)
#pragma unroll
    for (int q = 0; q < 8; ++q) {
#pragma unroll
      for (int o = 4; o < 64; o <<= 1) { ag[m][q] += __shfl_xor(ag[m][q], o); au[m][q] += __shfl_xor(au[m][q], o); }
      if (l < 4) { red[w][m][l * 8 + q] = ag[m][q]; red[w][2 + m][l * 8 + q] = au[m][q]; }
    }
  __syncthreads();
  if (tid < 64) {
    int m = tid >> 5, f = tid & 31;
    float gg = bfr(red[0][m][f] + red[1][m][f] + red[2][m][f] + red[3][m][f]);
    float uu = bfr(red[0][2 + m][f] + red[1][2 + m][f] + red[2][2 + m][f] + red[3][2 + m][f]);
    act[m * F + f0 + f] = bfr(bfr(silu(gg)) * uu);
  }
}

__global__ __launch_bounds__(256) void k6(const float* __restrict__ act, const float* __restrict__ sel, const u16* __restrict__ wd,
                                          const u16* __restrict__ x1, const u16* __restrict__ h2, const float* __restrict__ modp, u16* __restrict__ out) {
  __shared__ float red[4][2][16];
  const int tid = threadIdx.x, l = tid & 63, w = tid >> 6;
  const int rg = tid >> 1, cc = tid & 1, e0 = blockIdx.x * 16;
  float a[2][8] = {};
#pragma unroll
  for (int i = 0; i < 14; ++i) {
    int f = rg + 128 * i;
    u32x4 v = *(const u32x4*)(wd + (size_t)f * D + e0 + cc * 8);
    float h0 = act[f], h1 = act[F + f];
#pragma unroll
    for (int q = 0; q < 4; ++q) {
      a[0][2 * q] += h0 * lo16(v[q]); a[0][2 * q + 1] += h0 * hi16(v[q]);
      a[1][2 * q] += h1 * lo16(v[q]); a[1][2 * q + 1] += h1 * hi16(v[q]);
    }
  }
#pragma unroll
  for (int m = 0; m < 2; ++m)
#pragma unroll
    for (int q = 0; q < 8; ++q) {
#pragma unroll
      for (int o = 2; o < 64; o <<= 1) a[m][q] += __shfl_xor(a[m][q], o);
      if (l < 2) red[w][m][l * 8 + q] = a[m][q];
    }
  __syncthreads();
  if (tid < 32) {
    int m = tid >> 4, e = e0 + (tid & 15);
    int row = ((const int*)sel)[m]; float rv = sel[2 + m];
    float y = bfr(red[0][m][tid & 15] + red[1][m][tid & 15] + red[2][m][tid & 15] + red[3][m][tid & 15]);
    float sg = bfr(1.f / (1.f + __expf(-rv)));
    y = bfr(y * sg);
    float hb = bfr(bf2f(h2[(size_t)row * D + e]) + y);
    float o = bf2f(x1[(size_t)row * D + e]) + bfr(modv(modp, 5 * 512 + e) * hb);
    out[(size_t)row * D + e] = (u16)(pk2(o, o) & 0xffff);
  }
}

extern "C" void kernel_launch(void* const* d_in, const int* in_sizes, int n_in,
                              void* d_out, int out_size, void* d_ws, size_t ws_size,
                              hipStream_t stream) {
  const u16* x = (const u16*)d_in[0];
  const u16* Wre = (const u16*)d_in[1]; const u16* Wim = (const u16*)d_in[2];
  const u16* Dre = (const u16*)d_in[3]; const u16* Dim = (const u16*)d_in[4];
  const u16* wr = (const u16*)d_in[6]; const u16* wg = (const u16*)d_in[7]; const u16* wu = (const u16*)d_in[8];
  const u16* wd = (const u16*)d_in[9]; const u16* c = (const u16*)d_in[10]; const u16* wmod = (const u16*)d_in[11];
  char* ws = (char*)d_ws;
  u16* n8 = (u16*)(ws + WS_N8); u16* PQ = (u16*)(ws + WS_PQ); u16* x1 = (u16*)(ws + WS_X1);
  float* modp = (float*)(ws + WS_MOD); float* rr = (float*)(ws + WS_R); u16* h2 = (u16*)(ws + WS_H2);
  float* act = (float*)(ws + WS_ACT); float* sel = (float*)(ws + WS_SEL);
  u16* out = (u16*)d_out;
  (void)hipFuncSetAttribute((const void*)k2, hipFuncAttributeMaxDynamicSharedMemorySize, NST * 24576);
  (void)hipFuncSetAttribute((const void*)k3, hipFuncAttributeMaxDynamicSharedMemorySize, 8 * 16384);
  k1<<<448, 256, 0, stream>>>(x, c, wmod, n8, modp);
  k2<<<256, 256, NST * 24576, stream>>>(Wre, Wim, n8, modp, PQ);
  k3<<<256, 256, 8 * 16384, stream>>>(PQ, Dre, Dim, x, modp, x1);
  k4<<<512, 256, 0, stream>>>(x1, modp, wr, h2, rr, out);
  k5<<<56, 256, 0, stream>>>(rr, h2, wg, wu, act, sel);
  k6<<<32, 256, 0, stream>>>(act, sel, wd, x1, h2, modp, out);
}
```

```cpp
#include <hip/hip_runtime.h>
#include <hip/hip_bf16.h>
typedef unsigned short u16;
typedef unsigned long long u64;
typedef __attribute__((ext_vector_type(8))) short bf16x8;
typedef __attribute__((ext_vector_type(4))) short s16x4;
typedef __attribute__((ext_vector_type(16))) float f32x16;
typedef __attribute__((ext_vector_type(4))) float f32x4;
typedef __attribute__((ext_vector_type(2))) float f32x2;
typedef __attribute__((ext_vector_type(2))) unsigned u32x2;
typedef __attribute__((ext_vector_type(4))) unsigned u32x4;
typedef __attribute__((ext_vector_type(2))) __bf16 bf2_t;
#define LAS __attribute__((address_space(3)))
#define GAS __attribute__((address_space(1)))
#define DI __device__ __forceinline__

#define TH 8
#define DLY_N 12
#define DLY_P 12
#define DLY_S 12
constexpr int SPIN_CAP = 1 << 15;

constexpr unsigned WS_EPOCH = 0;
constexpr unsigned WS_FLAGN = 0x1000;
constexpr unsigned WS_FLAGP = 0x30000;
constexpr unsigned WS_MODG = 0x60000;
constexpr unsigned WS_STATG = 0x70000;
constexpr unsigned WS_CANDG = 0xF0000;
constexpr unsigned WS_ROWG = 0x100000;
constexpr unsigned WS_YG = 0x140000;
constexpr unsigned WS_N8 = 0x400000;
constexpr unsigned WS_PQ = 0x600000;
constexpr unsigned WS_END = 0xA00000;
constexpr unsigned WS_TS = 0xB00000;

constexpr int NA = 8, NB = 3;
constexpr int L_A = 32768;
constexpr int L_B = 8192;
constexpr int L_T = L_B;
constexpr int L_M = L_B + 12288;
constexpr int L_X = L_B;
constexpr int L_G = L_B + 8192;
constexpr int L_MISC = 0;
constexpr int L_MOD = L_MISC;
constexpr int L_UC = L_MISC + 2048;
constexpr int L_CNT = L_MISC + 2064;
constexpr int L_TAG = L_MISC + 2068;
constexpr int L_KEY = L_MISC + 2080;
constexpr int L_SEL = L_MISC + 2112;
constexpr int L_R = L_MISC + 2176;
constexpr int L_ACT = L_MISC + 3200;
constexpr int L_ROW = L_MISC + 3456;
constexpr int L_H = L_MISC + 4480;
constexpr int LDS_BYTES = 163840;

DI unsigned pk2(float a, float b) { unsigned r; asm("v_cvt_pk_bf16_f32 %0, %1, %2" : "=v"(r) : "v"(a), "v"(b)); return r; }
DI float bfr(float a) { return __uint_as_float(pk2(a, a) << 16); }
DI float lo16(unsigned u) { return __uint_as_float(u << 16); }
DI float hi16(unsigned u) { return __uint_as_float(u & 0xffff0000u); }
template<int N> DI void wait_vm() { asm volatile("s_waitcnt vmcnt(%0)" :: "n"(N) : "memory"); }
DI void wait_lgkm() { asm volatile("s_waitcnt lgkmcnt(0)" ::: "memory"); }
DI void bar() { asm volatile("s_barrier" ::: "memory"); }
DI void barl() { asm volatile("s_waitcnt lgkmcnt(0)\n\ts_barrier" ::: "memory"); }
DI float silu(float x) { return x * __builtin_amdgcn_rcpf(1.f + __expf(-x)); }
template<int C> DI unsigned dppu(unsigned v) { return (unsigned)__builtin_amdgcn_update_dpp(0, (int)v, C, 0xf, 0xf, true); }
template<int C> DI float dppf(float v) { return __uint_as_float(dppu<C>(__float_as_uint(v))); }
DI float sum_x16(float v) { auto r = __builtin_amdgcn_permlane16_swap(__float_as_uint(v), __float_as_uint(v), false, false); return __uint_as_float(r[0]) + __uint_as_float(r[1]); }
DI float sum_x32(float v) { auto r = __builtin_amdgcn_permlane32_swap(__float_as_uint(v), __float_as_uint(v), false, false); return __uint_as_float(r[0]) + __uint_as_float(r[1]); }
DI float sum_q(float v) { return sum_x32(sum_x16(v)); }
DI float sum_row(float v) { v += dppf<0xB1>(v); v += dppf<0x4E>(v); v += dppf<0x141>(v); v += dppf<0x140>(v); return v; }
DI float sum64(float v) { return sum_q(sum_row(v)); }
DI void top2(unsigned& a1, unsigned& a2, unsigned b1, unsigned b2) {
  unsigned m = max(a2, b2), hi = max(a1, b1), lo = min(a1, b1); a1 = hi; a2 = max(lo, m);
}
DI u32x4 mksrd(const void* p, unsigned bytes) { u64 a = (u64)p; u32x4 r = {(unsigned)a, (unsigned)(a >> 32) & 0xffffu, bytes, 0x00020000u}; return r; }
DI void dma(unsigned lds, unsigned voff, u32x4 srd, unsigned soff) {
  asm volatile("s_mov_b32 m0, %0\n\ts_nop 0\n\tbuffer_load_dwordx4 %1, %2, %3 offen lds" :: "s"(lds), "v"(voff), "s"(srd), "s"(soff) : "memory");
}
DI void dma_sc1(unsigned lds, unsigned voff, u32x4 srd, unsigned soff) {
  asm volatile("s_mov_b32 m0, %0\n\ts_nop 0\n\tbuffer_load_dwordx4 %1, %2, %3 offen sc1 lds" :: "s"(lds), "v"(voff), "s"(srd), "s"(soff) : "memory");
}
typedef short v4i16_t __attribute__((ext_vector_type(4)));
DI s16x4 trd(const char* p) { return __builtin_amdgcn_ds_read_tr16_b64_v4i16((LAS v4i16_t*)p); }
DI bf16x8 cat(s16x4 a, s16x4 b) { return __builtin_shufflevector(a, b, 0, 1, 2, 3, 4, 5, 6, 7); }

#define ST(i)

__global__ __launch_bounds__(512, 2) void fused(const u16* __restrict__ x, const u16* __restrict__ Wre, const u16* __restrict__ Wim,
                                                const u16* __restrict__ Dre, const u16* __restrict__ Dim, const u16* __restrict__ wr,
                                                const u16* __restrict__ wg, const u16* __restrict__ wu, const u16* __restrict__ wd,
                                                const u16* __restrict__ cvec, const u16* __restrict__ wmod, u16* __restrict__ out, char* __restrict__ ws) {
  __shared__ __attribute__((aligned(16))) char smem[LDS_BYTES];
  ST(0);
  const int tid = threadIdx.x, l = tid & 63;
  const int w = __builtin_amdgcn_readfirstlane(tid >> 6);
  const int b = blockIdx.x, g = b & 7, idx = b >> 3, pl = idx >> 3, j = idx & 7, p = 4 * g + pl;
  const __amdgpu_buffer_rsrc_t rws = __builtin_amdgcn_make_buffer_rsrc(ws, 0, WS_END, 0x00020000);
  const int kt0 = 8 * (((j >> 1) + pl) & 3);
  const int nprod = g < 4 ? 16 : 12;
  const bool is_prod = idx < nprod, is_red = idx >= 24;

  if (w >= 4) {
    const int lw = w - 4;
    const unsigned lds0 = (unsigned)(size_t)smem;
    const u32x4 srdA = mksrd((lw < 2 ? Wre : Wim) + (size_t)(64 * p + 32 * (lw & 1)) * 2048, 32 * 4096);
    const u32x4 srdB = mksrd(ws + WS_N8 + j * 1024, (2u << 20) - j * 1024);
    const unsigned vA0 = (l >> 3) * 4096 + (((l & 7) ^ (l >> 4)) << 4), vA1 = (vA0 ^ 64) + 8 * 4096;
    const unsigned vB = l * 16;
    auto issueA = [&](int s, int slot) {
      const unsigned so = ((s + kt0) & 31) * 128, d = lds0 + L_A + slot * 16384 + lw * 4096;
      dma(d, vA0, srdA, so); dma(d + 1024, vA1, srdA, so); dma(d + 2048, vA0, srdA, so + 16 * 4096); dma(d + 3072, vA1, srdA, so + 16 * 4096);
    };
    auto issueB = [&](int s, int slot) {
      const unsigned so = (((s + kt0) & 31) * 8 + 2 * lw) * 8192, d = lds0 + L_B + slot * 8192 + lw * 2048;
      dma_sc1(d, vB, srdB, so); dma_sc1(d + 1024, vB, srdB, so + 8192);
    };
#pragma unroll 1
    for (int s = 0; s < TH / 4; ++s) issueA(s, s);
    ST(1);
    bar();
#pragma unroll 1
    for (int s = TH / 4; s < NA - 1; ++s) { wait_vm<TH - 4>(); issueA(s, s); }
    ST(2);
    bar();
    ST(3);
#pragma unroll
    for (int s = 0; s < NB - 1; ++s) issueB(s, s);
    wait_vm<2 * (NB - 2)>();
    int sa = NA - 1, sb = NB - 1;
#pragma unroll 1
    for (int kt = 0; kt < 33 - NA; ++kt) {
      bar();
      issueA(kt + NA - 1, sa); issueB(kt + NB - 1, sb);
      sa = sa == NA - 1 ? 0 : sa + 1; sb = sb == NB - 1 ? 0 : sb + 1;
      static_assert(NB == 3, "wait counts");
      wait_vm<6>();
    }
#pragma unroll 1
    for (int kt = 33 - NA; kt < 33 - NB; ++kt) {
      bar();
      issueB(kt + NB - 1, sb); sb = sb == NB - 1 ? 0 : sb + 1;
      wait_vm<2 * (NB - 2)>();
    }
    wait_vm<0>();
#pragma unroll
    for (int kt = 33 - NB; kt < 32; ++kt) bar();
    ST(4);
    bar();
    {
      const unsigned vW0 = (l >> 3) * 1024 + (((l & 7) ^ (l >> 4)) << 4), vW1 = (vW0 ^ 64) + 8 * 1024;
      const u32x4 srdR = mksrd(Dre + (size_t)(64 * j + 16 * lw) * 512, 16 * 1024), srdI = mksrd(Dim + (size_t)(64 * j + 16 * lw) * 512, 16 * 1024);
#pragma unroll 1
      for (int i = 0; i < 16; ++i) {
        const int u = (i + 4 * pl) & 15;
        const u32x4 sr = u < 8 ? srdR : srdI;
        const unsigned d = lds0 + L_A + u * 8192 + lw * 2048;
        dma(d, vW0, sr, (u & 7) * 128); dma(d + 1024, vW1, sr, (u & 7) * 128);
      }
      const u32x4 srdX = mksrd(x + (size_t)(64 * p + 16 * lw) * 512 + 64 * j, 16 * 1024);
      dma(lds0 + L_X + lw * 2048, vW0, srdX, 0); dma(lds0 + L_X + lw * 2048 + 1024, vW1, srdX, 0);
      wait_vm<0>();
    }
    ST(5);
    bar();
    const unsigned tag = __builtin_amdgcn_readfirstlane(*(const unsigned*)(smem + L_TAG));
    bf16x8 fg[4], fu[4], fd[4];
    const int fs = (g + 8 * (idx >> 2)) * 4 + (idx & 3);
    if (is_prod) {
      u32x4 rg[4], ru[4], rd[4];
#pragma unroll
      for (int i = 0; i < 4; ++i) {
        const size_t o = (size_t)(128 * lw + 32 * i + (l >> 1)) * 1792 + 16 * fs + 8 * (l & 1);
        rg[i] = *(const u32x4*)(wg + o); ru[i] = *(const u32x4*)(wu + o);
        rd[i] = *(const u32x4*)(wd + (size_t)(16 * fs + 4 * i + (l >> 4)) * 512 + 128 * lw + 8 * (l & 15));
      }
      char* G = smem + L_G + lw * 4096;
      const int q4 = (l & 15) >> 2, p4 = l & 3;
#pragma unroll
      for (int i = 0; i < 4; ++i) *(u32x4*)(G + i * 1024 + l * 16) = rg[i];
      wait_lgkm();
#pragma unroll
      for (int i = 0; i < 4; ++i) { const char* a = G + (32 * i + 8 * (l >> 4) + q4) * 32 + p4 * 8; fg[i] = cat(trd(a), trd(a + 128)); }
      wait_lgkm();
#pragma unroll
      for (int i = 0; i < 4; ++i) *(u32x4*)(G + i * 1024 + l * 16) = ru[i];
      wait_lgkm();
#pragma unroll
      for (int i = 0; i < 4; ++i) { const char* a = G + (32 * i + 8 * (l >> 4) + q4) * 32 + p4 * 8; fu[i] = cat(trd(a), trd(a + 128)); }
      wait_lgkm();
#pragma unroll
      for (int i = 0; i < 4; ++i) *(u32x4*)(G + i * 1024 + l * 16) = rd[i];
      wait_lgkm();
#pragma unroll
      for (int i = 0; i < 4; ++i) { const char* a = G + (8 * (l >> 5) + q4) * 256 + (32 * i + 16 * ((l >> 4) & 1) + 4 * p4) * 2; fd[i] = cat(trd(a), trd(a + 1024)); }
      wait_lgkm();
    }
    ST(6);
    bar();
    ST(7);
    unsigned k1 = 0, k2 = 0;
    if (is_prod || lw == 0) {
      __builtin_amdgcn_s_sleep(DLY_S);
      _Pragma("unroll 1") for (int spin = 0; spin < SPIN_CAP; ++spin) {
        asm volatile("" ::: "memory");
        const u32x4 v = __builtin_amdgcn_raw_buffer_load_b128(rws, WS_CANDG + (l & 31) * 128, 0, 16);
        k1 = v[0]; k2 = v[2];
        if (__all(v[1] == tag && v[3] == tag)) break;
        __builtin_amdgcn_s_sleep(1);
      }
      top2(k1, k2, dppu<0xB1>(k1), dppu<0xB1>(k2)); top2(k1, k2, dppu<0x4E>(k1), dppu<0x4E>(k2));
      top2(k1, k2, dppu<0x141>(k1), dppu<0x141>(k2)); top2(k1, k2, dppu<0x140>(k1), dppu<0x140>(k2));
      { unsigned a1 = __builtin_amdgcn_readlane(k1, 0), a2 = __builtin_amdgcn_readlane(k2, 0), b1 = __builtin_amdgcn_readlane(k1, 16), b2 = __builtin_amdgcn_readlane(k2, 16);
        top2(a1, a2, b1, b2); k1 = a1; k2 = a2; }
      if (lw == 0 && l == 0) *(u32x2*)(smem + L_SEL) = u32x2{k1, k2};
    }
    ST(8);
    const int r1 = 2047 - (k1 & 2047), r2 = 2047 - (k2 & 2047);
    const unsigned slot1 = (r1 >> 6) * 2, slot2 = (r2 >> 6) * 2 + ((r1 >> 6) == (r2 >> 6));
    if (is_prod) {
      u32x4 v0, v1;
      _Pragma("unroll 1") for (int spin = 0; spin < SPIN_CAP; ++spin) {
        asm volatile("" ::: "memory");
        v0 = __builtin_amdgcn_raw_buffer_load_b128(rws, l * 16, WS_ROWG + slot1 * 4096 + lw * 1024, 16);
        v1 = __builtin_amdgcn_raw_buffer_load_b128(rws, l * 16, WS_ROWG + slot2 * 4096 + lw * 1024, 16);
        if (__all(v0[1] == tag && v0[3] == tag && v1[1] == tag && v1[3] == tag)) break;
        __builtin_amdgcn_s_sleep(1);
      }
      ST(9);
      char* H = smem + L_H + lw * 512;
      *(unsigned*)(H + l * 4) = (v0[0] & 0xffffu) | (v0[2] << 16);
      *(unsigned*)(H + 256 + l * 4) = (v1[0] & 0xffffu) | (v1[2] << 16);
      wait_lgkm();
      f32x4 ag = {}, au = {};
#pragma unroll
      for (int i = 0; i < 4; ++i) {
        const bf16x8 a = *(const bf16x8*)(H + (l & 1) * 256 + i * 64 + (l >> 4) * 16);
        ag = __builtin_amdgcn_mfma_f32_16x16x32_bf16(a, fg[i], ag, 0, 0, 0);
        au = __builtin_amdgcn_mfma_f32_16x16x32_bf16(a, fu[i], au, 0, 0, 0);
      }
      if (l < 16) *(f32x4*)(smem + L_R + lw * 256 + l * 16) = f32x4{ag[0], au[0], ag[1], au[1]};
    }
    barl();
    ST(10);
    if (is_prod) {
      f32x2 s = {0.f, 0.f};
#pragma unroll
      for (int i = 0; i < 4; ++i) s += *(const f32x2*)(smem + L_R + i * 256 + (l & 15) * 16 + ((l >> 4) & 1) * 8);
      const float a = silu(s[0]) * s[1];
      char* A = smem + L_ACT + lw * 64;
      *(u16*)(A + (l & 31) * 2) = (u16)pk2(a, a);
      wait_lgkm();
      const bf16x8 af = *(const bf16x8*)(A + (l & 1) * 32 + (l >> 5) * 16);
#pragma unroll
      for (int i = 0; i < 4; i += 2) {
        f32x16 y0 = {}, y1 = {};
        y0 = __builtin_amdgcn_mfma_f32_32x32x16_bf16(af, fd[i], y0, 0, 0, 0);
        y1 = __builtin_amdgcn_mfma_f32_32x32x16_bf16(af, fd[i + 1], y1, 0, 0, 0);
        const u32x4 v = {__float_as_uint(l < 32 ? y0[0] : y1[0]), tag, __float_as_uint(l < 32 ? y0[1] : y1[1]), tag};
        __builtin_amdgcn_raw_buffer_store_b128(v, rws, l * 16, WS_YG + fs * 8192 + (128 * lw + 32 * i) * 16, 16);
      }
    }
    if (is_red) {
      const u32x2 sel = *(const u32x2*)(smem + L_SEL);
      const int m = (l >> 3) & 1, e = 64 * j + 8 * g + (l & 7);
      const unsigned km = m ? sel[1] : sel[0];
      const int q1 = 2047 - (sel[0] & 2047), q2 = 2047 - (sel[1] & 2047);
      const unsigned sl = m ? (q2 >> 6) * 2 + ((q1 >> 6) == (q2 >> 6)) : (q1 >> 6) * 2;
      u32x2 hx = {0, 0};
      if (lw == 0) {
        _Pragma("unroll 1") for (int spin = 0; spin < SPIN_CAP; ++spin) {
          asm volatile("" ::: "memory");
          hx = __builtin_amdgcn_raw_buffer_load_b64(rws, WS_ROWG + sl * 4096 + e * 8, 0, 16);
          if (__all(hx[1] == tag)) break;
          __builtin_amdgcn_s_sleep(1);
        }
      }
      __builtin_amdgcn_s_sleep(DLY_S);
      float y0, y1;
      const unsigned vo = WS_YG + ((l >> 3) + 8 * lw) * 8192 + (64 * j + 8 * g + (l & 7)) * 16;
      _Pragma("unroll 1") for (int spin = 0; spin < SPIN_CAP; ++spin) {
        asm volatile("" ::: "memory");
        bool ok = true; y0 = 0.f; y1 = 0.f;
#pragma unroll
        for (int i = 0; i < 4; ++i) {
          if (i < 3 || lw < 2) {
            const u32x4 v = __builtin_amdgcn_raw_buffer_load_b128(rws, vo, i * 32 * 8192, 16);
            ok &= v[1] == tag && v[3] == tag; y0 += __uint_as_float(v[0]); y1 += __uint_as_float(v[2]);
          }
        }
        if (__all(ok)) break;
        __builtin_amdgcn_s_sleep(1);
      }
      ST(11);
      y0 += dppf<0x128>(y0); y1 += dppf<0x128>(y1);
      const float y = sum_q(m ? y1 : y0);
      if (l < 16) *(float*)(smem + L_R + lw * 64 + l * 4) = y;
      barl();
      if (lw == 0 && l < 16) {
        const float* R = (const float*)(smem + L_R) + l;
        const float yt = (R[0] + R[16]) + (R[32] + R[48]);
        const unsigned ku = km & ~2047u;
        const float rv = __uint_as_float((ku & 0x80000000u) ? (ku & 0x7fffffffu) : ~(ku | 2047u));
        const float sg = __builtin_amdgcn_rcpf(1.f + __expf(-rv));
        const float gb = *(const float*)(smem + L_MOD + 5 * 256 + (8 * g + (l & 7)) * 4);
        const float o = hi16(hx[0]) + gb * (lo16(hx[0]) + yt * sg);
        out[(size_t)(m ? q2 : q1) * 512 + e] = (u16)pk2(o, o);
      }
    }
  } else {
    const bool do_mod = idx < 24;
    const int mu = g * 24 + idx, kc = mu >= 96, cc = mu - 96 * kc;
    unsigned tag;
    {
      const u32x4 xa = *(const u32x4*)(x + (size_t)(8 * b + 2 * w) * 512 + 8 * l);
      const u32x4 xb = *(const u32x4*)(x + (size_t)(8 * b + 2 * w + 1) * 512 + 8 * l);
      const unsigned ep = __builtin_amdgcn_raw_buffer_load_b32(rws, 0, WS_EPOCH, 16);
      const __amdgpu_buffer_rsrc_t rwm = __builtin_amdgcn_make_buffer_rsrc((void*)wmod, 0, do_mod ? 512 * 3072 * 2 : 0, 0x00020000);
      const __amdgpu_buffer_rsrc_t rcv = __builtin_amdgcn_make_buffer_rsrc((void*)cvec, 0, do_mod ? 1024 : 0, 0x00020000);
      u32x4 wv[4]; unsigned cv[4];
#pragma unroll
      for (int i = 0; i < 4; ++i) {
        wv[i] = __builtin_amdgcn_raw_buffer_load_b128(rwm, (l >> 2) * 6144 + (l & 3) * 16, (kc * 256 + 64 * w + 16 * i) * 6144 + cc * 64, 0);
        cv[i] = __builtin_amdgcn_raw_buffer_load_b16(rcv, (l >> 2) * 2 + 32 * i, (kc * 256 + 64 * w) * 2, 0);
      }
      const unsigned wrv = wr[64 * j + l];
      float a[8], c[8], sa_ = 0.f, sc_ = 0.f;
#pragma unroll
      for (int i = 0; i < 4; ++i) { a[2 * i] = lo16(xa[i]); a[2 * i + 1] = hi16(xa[i]); c[2 * i] = lo16(xb[i]); c[2 * i + 1] = hi16(xb[i]); }
#pragma unroll
      for (int i = 0; i < 8; ++i) { sa_ += a[i]; sc_ += c[i]; }
      ST(1);
      const float ma = sum64(sa_) * (1.f / 512), mc = sum64(sc_) * (1.f / 512);
      sa_ = 0.f; sc_ = 0.f;
#pragma unroll
      for (int i = 0; i < 8; ++i) { a[i] -= ma; c[i] -= mc; sa_ += a[i] * a[i]; sc_ += c[i] * c[i]; }
      const float ra = __builtin_amdgcn_rsqf(sum64(sa_) * (1.f / 512) + 1e-5f), rc = __builtin_amdgcn_rsqf(sum64(sc_) * (1.f / 512) + 1e-5f);
#pragma unroll
      for (int i = 0; i < 8; ++i) *(unsigned*)(smem + L_T + w * 2176 + (i * 68 + l) * 4) = pk2(a[i] * ra, c[i] * rc);
      tag = __builtin_amdgcn_readfirstlane(ep) + 1u;
      if (tid == 0) *(u32x2*)(smem + L_CNT) = u32x2{0u, tag};
      barl();
#pragma unroll
      for (int h = 0; h < 2; ++h) {
        const int d = tid + 256 * h; const unsigned* T = (const unsigned*)(smem + L_T) + (d & 7) * 68 + (d >> 3);
        const u32x4 v = {T[0], T[544], T[1088], T[1632]};
        __builtin_amdgcn_raw_buffer_store_b128(v, rws, d * 16, WS_N8 + b * 8192, 16);
      }
      ST(2);
      unsigned last = 0;
      if (w == 3) *(float*)(smem + L_MOD + 6 * 256 + l * 4) = lo16(wrv);
      if (do_mod) {
        float acc[8] = {};
#pragma unroll
        for (int i = 0; i < 4; ++i) {
          const float s = bfr(silu(lo16(cv[i])));
#pragma unroll
          for (int q = 0; q < 4; ++q) { acc[2 * q] += s * lo16(wv[i][q]); acc[2 * q + 1] += s * hi16(wv[i][q]); }
        }
#pragma unroll
        for (int q = 0; q < 8; ++q) { acc[q] += dppf<0x124>(acc[q]); acc[q] += dppf<0x128>(acc[q]); }
        if ((l & 15) < 4) {
          f32x4* M = (f32x4*)(smem + L_M + (w * 4 + (l >> 4)) * 128 + (l & 3) * 32);
          M[0] = f32x4{acc[0], acc[1], acc[2], acc[3]}; M[1] = f32x4{acc[4], acc[5], acc[6], acc[7]};
        }
        wait_lgkm();
        if (l == 0) last = __hip_atomic_fetch_add((LAS unsigned*)(smem + L_CNT), 1u, __ATOMIC_RELAXED, __HIP_MEMORY_SCOPE_WORKGROUP);
        last = __builtin_amdgcn_readfirstlane(last) == 3;
      }
      wait_vm<0>();
      if (l == 0) __builtin_amdgcn_raw_buffer_store_b32(tag, rws, WS_FLAGN + (w * 256 + b) * 128, 0, 16);
      ST(3);
      if (last) {
        float s = 0.f;
#pragma unroll
        for (int i = 0; i < 16; ++i) s += *(const float*)(smem + L_M + i * 128 + (l & 31) * 4);
        if (l < 32) __builtin_amdgcn_raw_buffer_store_b64(u32x2{__float_as_uint(s), tag}, rws, WS_MODG + (kc * 3072 + cc * 32 + l) * 8, 0, 16);
      }
    }
    {
      __builtin_amdgcn_s_sleep(DLY_N);
      const int which = tid >> 5;
      const unsigned mo = WS_MODG + (unsigned)((w < 3 ? which : 0) * 512 + 64 * j + 2 * (tid & 31)) * 8;
      u32x4 m0, m1;
      _Pragma("unroll 1") for (int spin = 0; spin < SPIN_CAP; ++spin) {
        asm volatile("" ::: "memory");
        const unsigned f = __builtin_amdgcn_raw_buffer_load_b32(rws, WS_FLAGN + ((j & 3) * 256 + tid) * 128, 0, 16);
        m0 = __builtin_amdgcn_raw_buffer_load_b128(rws, mo, 0, 16);
        m1 = __builtin_amdgcn_raw_buffer_load_b128(rws, mo, 3072 * 8, 16);
        if (__all(f == tag && m0[1] == tag && m0[3] == tag && m1[1] == tag && m1[3] == tag)) break;
        __builtin_amdgcn_s_sleep(1);
      }
      ST(4);
      bar();
      if (w < 3) {
        float v0 = bfr(__uint_as_float(m0[0]) + __uint_as_float(m1[0])), v1 = bfr(__uint_as_float(m0[2]) + __uint_as_float(m1[2]));
        const float one = (which == 1 || which == 4) ? 1.f : 0.f;
        v0 = bfr(one + v0); v1 = bfr(one + v1);
        *(f32x2*)(smem + L_MOD + which * 256 + (tid & 31) * 8) = f32x2{v0, v1};
      } else if (b == 0 && l == 0) {
        __builtin_amdgcn_raw_buffer_store_b32(tag, rws, 0, WS_EPOCH, 16);
      }
      wait_lgkm();
    }
    const int h = l >> 5, r = l & 31;
    f32x16 acc0 = {}, acc1 = {};
    float rsum = 0.f;
    {
      const int sw = (r >> 1) & 7;
      const int ao = L_A + w * 4096 + r * 128, bo = L_B + h * 1024 + r * 16;
      int sa = 0, sb = 0;
#pragma unroll 1
      for (int kt = 0; kt < 32; ++kt) {
        bar();
        const char* pa = smem + ao + sa * 16384; const char* pb = smem + bo + sb * 8192;
        bf16x8 af[4], b0[4], b1[4];
#pragma unroll
        for (int kk = 0; kk < 4; ++kk) {
          af[kk] = *(const bf16x8*)(pa + (((2 * kk + h) ^ sw) << 4));
          b0[kk] = *(const bf16x8*)(pb + kk * 2048);
          b1[kk] = *(const bf16x8*)(pb + kk * 2048 + 512);
        }
        __builtin_amdgcn_sched_barrier(0);
#pragma unroll
        for (int kk = 0; kk < 4; ++kk) {
          acc0 = __builtin_amdgcn_mfma_f32_32x32x16_bf16(b0[kk], af[kk], acc0, 0, 0, 0);
          acc1 = __builtin_amdgcn_mfma_f32_32x32x16_bf16(b1[kk], af[kk], acc1, 0, 0, 0);
          const u32x4 u = __builtin_bit_cast(u32x4, af[kk]);
#pragma unroll
          for (int q = 0; q < 4; ++q) { const unsigned uq = u[q]; rsum = __builtin_amdgcn_fdot2_f32_bf16(__builtin_bit_cast(bf2_t, uq), __builtin_bit_cast(bf2_t, 0x3f803f80u), rsum, false); }
        }
        sa = sa == NA - 1 ? 0 : sa + 1; sb = sb == NB - 1 ? 0 : sb + 1;
      }
    }
    ST(5);
    {
      rsum = sum_x32(rsum);
      const unsigned sgn = w < 2 ? 0u : 0x80008000u;
      const unsigned po = WS_PQ + ((p * 4 + 2 * (w & 1) + (r >> 4)) * 128 + 64 * (w >> 1) + 8 * j + h) * 256 + (r & 15) * 16;
#pragma unroll
      for (int db = 0; db < 2; ++db)
#pragma unroll
        for (int q = 0; q < 4; q += 2) {
          unsigned pkd[4];
#pragma unroll
          for (int z = 0; z < 2; ++z) {
            const f32x4 sa4 = *(const f32x4*)(smem + L_MOD + (32 * db + 8 * (q + z) + 4 * h) * 4);
            const f32x4 ca4 = *(const f32x4*)(smem + L_MOD + 256 + (32 * db + 8 * (q + z) + 4 * h) * 4);
            float o[4];
#pragma unroll
            for (int e = 0; e < 4; ++e) o[e] = __builtin_fmaf(db ? acc1[4 * (q + z) + e] : acc0[4 * (q + z) + e], ca4[e], rsum * sa4[e]);
            pkd[2 * z] = pk2(o[0], o[1]) ^ sgn; pkd[2 * z + 1] = pk2(o[2], o[3]) ^ sgn;
          }
          const auto s0 = __builtin_amdgcn_permlane32_swap(pkd[0], pkd[2], false, false);
          const auto s1 = __builtin_amdgcn_permlane32_swap(pkd[1], pkd[3], false, false);
          __builtin_amdgcn_raw_buffer_store_b128(u32x4{s0[0], s1[0], s0[1], s1[1]}, rws, po, (4 * db + q) * 256, 16);
        }
    }
    bar();
    if (w == 0) {
      const float* M = (const float*)(smem + L_MOD);
      const float u = M[4 * 64 + l] * M[6 * 64 + l];
      *(float*)(smem + L_MOD + 7 * 256 + l * 4) = u;
      const float U = sum64(u), C = sum64(M[3 * 64 + l] * M[6 * 64 + l]);
      if (l == 0) *(f32x2*)(smem + L_UC) = f32x2{U, C};
    }
    wait_vm<0>();
    if (l == 0) __builtin_amdgcn_raw_buffer_store_b32(tag, rws, WS_FLAGP + (b * 4 + w) * 128, 0, 16);
    ST(6);
    const int t = l & 15, q = l >> 4;
    f32x4 ac[4] = {};
    {
      __builtin_amdgcn_s_sleep(DLY_P);
      const unsigned fo = WS_FLAGP + ((g + 8 * (8 * pl + (l & 7))) * 4 + (w >> 1) + 2 * ((l >> 3) & 1)) * 128;
      _Pragma("unroll 1") for (int spin = 0; spin < SPIN_CAP; ++spin) {
        asm volatile("" ::: "memory");
        if (__all(__builtin_amdgcn_raw_buffer_load_b32(rws, fo, 0, 16) == tag)) break;
        __builtin_amdgcn_s_sleep(1);
      }
      asm volatile("" ::: "memory");
      ST(7);
      u32x4 pf[32];
#pragma unroll
      for (int i = 0; i < 32; ++i) pf[i] = __builtin_amdgcn_raw_buffer_load_b128(rws, l * 16, WS_PQ + (p * 4 + w) * 32768 + ((i + 4 * j) & 31) * 1024, 16);
      barl();
      ST(8);
      const int wo = L_A + t * 128 + ((q ^ (t >> 1)) << 4);
#pragma unroll
      for (int i = 0; i < 32; ++i) {
        const char* pw = smem + ((wo ^ ((i & 1) << 6)) + (((i >> 1) + 2 * j) & 15) * 8192);
#pragma unroll
        for (int eb = 0; eb < 4; ++eb)
          ac[eb] = __builtin_amdgcn_mfma_f32_16x16x32_bf16(*(const bf16x8*)(pw + eb * 2048), __builtin_bit_cast(bf16x8, pf[i]), ac[eb], 0, 0, 0);
      }
    }
    ST(9);
    float x1[16];
    {
      float s = 0.f;
#pragma unroll
      for (int eb = 0; eb < 4; ++eb) {
        const u32x2 xv = *(const u32x2*)(smem + L_X + (16 * w + t) * 128 + (((2 * eb + (q >> 1)) ^ (t >> 1)) << 4) + (q & 1) * 8);
        const f32x4 ga = *(const f32x4*)(smem + L_MOD + 2 * 256 + (16 * eb + 4 * q) * 4);
        const unsigned u0 = pk2(lo16(xv[0]) + ga[0] * ac[eb][0], hi16(xv[0]) + ga[1] * ac[eb][1]);
        const unsigned u1 = pk2(lo16(xv[1]) + ga[2] * ac[eb][2], hi16(xv[1]) + ga[3] * ac[eb][3]);
        x1[4 * eb] = lo16(u0); x1[4 * eb + 1] = hi16(u0); x1[4 * eb + 2] = lo16(u1); x1[4 * eb + 3] = hi16(u1);
        s += (x1[4 * eb] + x1[4 * eb + 1]) + (x1[4 * eb + 2] + x1[4 * eb + 3]);
      }
      const float mj = sum_q(s) * (1.f / 64);
      float m2 = 0.f, s3 = 0.f;
#pragma unroll
      for (int eb = 0; eb < 4; ++eb) {
        const f32x4 u4 = *(const f32x4*)(smem + L_MOD + 7 * 256 + (16 * eb + 4 * q) * 4);
#pragma unroll
        for (int e = 0; e < 4; ++e) { const float d = x1[4 * eb + e] - mj; m2 += d * d; s3 += d * u4[e]; }
      }
      m2 = sum_q(m2); s3 = sum_q(s3);
      const float uc = *(const float*)(smem + L_UC + (t & 1) * 4);
      const float pv = q == 0 ? mj : q == 1 ? m2 : q == 2 ? s3 : uc;
      __builtin_amdgcn_raw_buffer_store_b64(u32x2{__float_as_uint(pv), tag}, rws, WS_STATG + (b * 4 + w) * 512 + l * 8, 0, 16);
    }
    ST(10);
    float mean, rs;
    unsigned key;
    {
      __builtin_amdgcn_s_sleep(DLY_S);
      float v[8];
      const unsigned so = WS_STATG + ((g + 64 * pl) * 4 + w) * 512 + l * 8;
      _Pragma("unroll 1") for (int spin = 0; spin < SPIN_CAP; ++spin) {
        asm volatile("" ::: "memory");
        bool ok = true;
#pragma unroll
        for (int i = 0; i < 8; ++i) { const u32x2 z = __builtin_amdgcn_raw_buffer_load_b64(rws, so, i * 8 * 2048, 16); v[i] = __uint_as_float(z[0]); ok &= z[1] == tag; }
        if (__all(ok)) break;
        __builtin_amdgcn_s_sleep(1);
      }
      ST(11);
      const float sum = ((v[0] + v[1]) + (v[2] + v[3])) + ((v[4] + v[5]) + (v[6] + v[7]));
      const float mu_ = sum * 0.125f;
      float dev2 = 0.f, devu = 0.f;
#pragma unroll
      for (int i = 0; i < 8; ++i) { const float d = v[i] - mu_; dev2 += d * d; devu += d * __uint_as_float(__builtin_amdgcn_readlane(__float_as_uint(v[i]), 48)); }
      mean = sum_q(q == 0 ? mu_ : 0.f);
      const float M2 = sum_q(q == 0 ? 64.f * dev2 : q == 1 ? sum : 0.f);
      const float B3 = sum_q(q == 0 ? devu : q == 2 ? sum : 0.f);
      rs = __builtin_amdgcn_rsqf(M2 * (1.f / 512) + 1e-5f);
      const float rv = B3 * rs + __uint_as_float(__builtin_amdgcn_readlane(__float_as_uint(sum), 49));
      unsigned u = __float_as_uint(rv); u = (u & 0x80000000u) ? ~u : (u | 0x80000000u);
      key = (u & ~2047u) | (2047 - (64 * p + 16 * w + t));
      unsigned k1 = key, k2 = 0;
      top2(k1, k2, dppu<0xB1>(k1), 0); top2(k1, k2, dppu<0x4E>(k1), dppu<0x4E>(k2));
      top2(k1, k2, dppu<0x141>(k1), dppu<0x141>(k2)); top2(k1, k2, dppu<0x140>(k1), dppu<0x140>(k2));
      if (l == 0) *(u32x2*)(smem + L_KEY + w * 8) = u32x2{k1, k2};
    }
    barl();
    {
      const u32x4 ka = *(const u32x4*)(smem + L_KEY), kb = *(const u32x4*)(smem + L_KEY + 16);
      unsigned k1 = ka[0], k2 = ka[1], k3 = kb[0], k4 = kb[1];
      top2(k1, k2, ka[2], ka[3]); top2(k3, k4, kb[2], kb[3]); top2(k1, k2, k3, k4);
      if (j == 0 && tid == 0) __builtin_amdgcn_raw_buffer_store_b128(u32x4{k1, tag, k2, tag}, rws, WS_CANDG + p * 128, 0, 16);
      ST(12);
      const bool cand = key == k1 || key == k2;
      unsigned o[8], hx[16];
#pragma unroll
      for (int eb = 0; eb < 4; ++eb) {
        const f32x4 sb = *(const f32x4*)(smem + L_MOD + 3 * 256 + (16 * eb + 4 * q) * 4);
        const f32x4 cb = *(const f32x4*)(smem + L_MOD + 4 * 256 + (16 * eb + 4 * q) * 4);
        const f32x4 gb = *(const f32x4*)(smem + L_MOD + 5 * 256 + (16 * eb + 4 * q) * 4);
        float ov[4];
#pragma unroll
        for (int e = 0; e < 4; ++e) {
          const float xv = x1[4 * eb + e];
          hx[4 * eb + e] = pk2((xv - mean) * rs * cb[e] + sb[e], xv);
          ov[e] = xv + gb[e] * lo16(hx[4 * eb + e]);
        }
        o[2 * eb] = pk2(ov[0], ov[1]); o[2 * eb + 1] = pk2(ov[2], ov[3]);
      }
#pragma unroll 1
      for (int m = 0; m < 2; ++m) {
        const unsigned km = __builtin_amdgcn_readfirstlane(m ? k2 : k1);
        if ((((2047 - (km & 2047)) >> 4) & 3) == (unsigned)w) {
          char* R = smem + L_ROW + w * 256;
          if (key == km) {
#pragma unroll
            for (int eb = 0; eb < 4; ++eb) *(u32x4*)(R + (16 * eb + 4 * q) * 4) = u32x4{hx[4 * eb], hx[4 * eb + 1], hx[4 * eb + 2], hx[4 * eb + 3]};
          }
          wait_lgkm();
          const u32x2 z = *(const u32x2*)(R + (l & 31) * 8);
          if (l < 32) __builtin_amdgcn_raw_buffer_store_b128(u32x4{z[0], tag, z[1], tag}, rws, WS_ROWG + (p * 2 + m) * 4096 + j * 512 + l * 16, 0, 16);
          wait_lgkm();
        }
      }
      u16* orow = out + (size_t)(64 * p + 16 * w + t) * 512 + 64 * j + 4 * q;
      if (!cand) {
#pragma unroll
        for (int eb = 0; eb < 4; ++eb) *(u32x2*)(orow + 16 * eb) = u32x2{o[2 * eb], o[2 * eb + 1]};
      }
      ST(13);
      bar();
      const u32x2 sel = *(const u32x2*)(smem + L_SEL);
      if (cand && key != sel[0] && key != sel[1]) {
#pragma unroll
        for (int eb = 0; eb < 4; ++eb) *(u32x2*)(orow + 16 * eb) = u32x2{o[2 * eb], o[2 * eb + 1]};
      }
    }
  }
}

extern "C" void kernel_launch(void* const* d_in, const int* in_sizes, int n_in,
                              void* d_out, int out_size, void* d_ws, size_t ws_size,
                              hipStream_t stream) {
  fused<<<256, 512, 0, stream>>>((const u16*)d_in[0], (const u16*)d_in[1], (const u16*)d_in[2], (const u16*)d_in[3], (const u16*)d_in[4],
                                 (const u16*)d_in[6], (const u16*)d_in[7], (const u16*)d_in[8], (const u16*)d_in[9], (const u16*)d_in[10],
                                 (const u16*)d_in[11], (u16*)d_out, (char*)d_ws);
}
```

```cpp
#include <hip/hip_runtime.h>
#include <hip/hip_bf16.h>
typedef unsigned short u16;
typedef unsigned long long u64;
typedef __attribute__((ext_vector_type(8))) short bf16x8;
typedef __attribute__((ext_vector_type(4))) short s16x4;
typedef __attribute__((ext_vector_type(4))) float f32x4;
typedef __attribute__((ext_vector_type(2))) float f32x2;
typedef __attribute__((ext_vector_type(2))) unsigned u32x2;
typedef __attribute__((ext_vector_type(4))) unsigned u32x4;
typedef __attribute__((ext_vector_type(2))) __bf16 bf2_t;
#define LAS __attribute__((address_space(3)))
#define DI __device__ __forceinline__

#define NB1 4
#define NB2 6
#define SCHED 0
#define LPRIO 0
#define T_POLL 360
#define DLY_S 14
#define DLY_C 12
#define DLY_Y 34
#define XNT
#define WNT 0
#define WTAIL 2
#define KT_RS 27
#define LASTNT 3
#define W29 2
#define W30 5
#define W31 8
#define DLY_P 16
#define DLY_L 2
#define UP_EARLY 0
#define DLY_LAZY 40
#define DLY_O 14
#define G_EARLY 2
#define NAP_S 2
constexpr int SPIN_CAP = 1 << 15;

constexpr unsigned WS_EPOCH = 0;
constexpr unsigned WS_FLAGN = 0x1000;
constexpr unsigned WS_FLAGP = 0x8000;
constexpr unsigned WS_MODG = 0x50000;
constexpr unsigned WS_STATG = 0x70000;
constexpr unsigned WS_CANDG = 0xF0000;
constexpr unsigned WS_RSG = 0x300000;
constexpr unsigned WS_ROWG = 0xA00000;
constexpr unsigned WS_YG = 0x140000;
constexpr unsigned WS_N8 = 0x400000;
constexpr unsigned WS_PQ = 0x600000;
constexpr unsigned WS_END = 0xC00000;
constexpr unsigned WS_TS = 0xD00000;

constexpr int NA = 7;
constexpr int L_MOD = 0;
constexpr int L_UC = 2048;
constexpr int L_TAG = 2064;
constexpr int L_KEY = 2080;
constexpr int L_SEL = 2112;
constexpr int L_CNT = 2128;
constexpr int L_R = 2176;
constexpr int L_ROW = 3456;
constexpr int L_H = 5504;
constexpr int L_JUNK = 7168;
constexpr int L_X = 8192;
constexpr int L_B = 16384;
constexpr int L_T = L_B;
constexpr int L_M = L_B + 12288;
constexpr int L_G = L_B;
constexpr int L_W = 32768;
constexpr int L_A = 49152;
constexpr int LDS_BYTES = 163840;

DI unsigned pk2(float a, float b) { const bf2_t r = __builtin_convertvector(f32x2{a, b}, bf2_t); return __builtin_bit_cast(unsigned, r); }
DI float bfr(float a) { return __uint_as_float(pk2(a, a) << 16); }
DI float lo16(unsigned u) { return __uint_as_float(u << 16); }
DI float hi16(unsigned u) { return __uint_as_float(u & 0xffff0000u); }
DI float asf(unsigned u) { return __uint_as_float(u); }
DI unsigned asu(float f) { return __float_as_uint(f); }
template<int N> DI void wait_vm() { asm volatile("s_waitcnt vmcnt(%0)" :: "n"(N) : "memory"); }
DI void wait_lgkm() { asm volatile("s_waitcnt lgkmcnt(0)" ::: "memory"); }
DI void bar() { asm volatile("s_barrier" ::: "memory"); }
DI void barl() { asm volatile("s_waitcnt lgkmcnt(0)\n\ts_barrier" ::: "memory"); }
DI void fence() { __builtin_amdgcn_sched_barrier(0); }
DI unsigned lds_arrive(char* p) { return __builtin_amdgcn_readfirstlane(__hip_atomic_fetch_add((LAS unsigned*)p, 1u, __ATOMIC_RELAXED, __HIP_MEMORY_SCOPE_WORKGROUP)); }
template<int NAP = 0> DI void lds_wait(char* p, unsigned n) {
#pragma unroll 1
  for (int spin = 0; spin < (SPIN_CAP << 6); ++spin) {
    if ((unsigned)__builtin_amdgcn_readfirstlane(__hip_atomic_load((LAS unsigned*)p, __ATOMIC_RELAXED, __HIP_MEMORY_SCOPE_WORKGROUP)) >= n) break;
    if (NAP) __builtin_amdgcn_s_sleep(NAP);
  }
  asm volatile("" ::: "memory");
}
DI bool none(unsigned bad) { return __builtin_amdgcn_ballot_w64(bad != 0) == 0; }
#define TAG2(v) (((v)[1] ^ tag) | ((v)[3] ^ tag))
DI float silu(float x) { return x * __builtin_amdgcn_rcpf(1.f + __expf(-x)); }
template<int C> DI unsigned dppu(unsigned v) { return (unsigned)__builtin_amdgcn_update_dpp(0, (int)v, C, 0xf, 0xf, true); }
template<int C> DI float dppf(float v) { return asf(dppu<C>(asu(v))); }
DI float sum_x16(float v) { auto r = __builtin_amdgcn_permlane16_swap(asu(v), asu(v), false, false); return asf(r[0]) + asf(r[1]); }
DI float sum_x32(float v) { auto r = __builtin_amdgcn_permlane32_swap(asu(v), asu(v), false, false); return asf(r[0]) + asf(r[1]); }
DI float sum_q(float v) { return sum_x32(sum_x16(v)); }
DI float sum_row(float v) { v += dppf<0xB1>(v); v += dppf<0x4E>(v); v += dppf<0x141>(v); v += dppf<0x140>(v); return v; }
DI float sum64(float v) { return sum_q(sum_row(v)); }
DI void top2(unsigned& a1, unsigned& a2, unsigned b1, unsigned b2) {
  unsigned m = max(a2, b2), hi = max(a1, b1), lo = min(a1, b1); a1 = hi; a2 = max(lo, m);
}
DI void top2_row(unsigned& k1, unsigned& k2) {
  top2(k1, k2, dppu<0xB1>(k1), dppu<0xB1>(k2)); top2(k1, k2, dppu<0x4E>(k1), dppu<0x4E>(k2));
  top2(k1, k2, dppu<0x141>(k1), dppu<0x141>(k2)); top2(k1, k2, dppu<0x140>(k1), dppu<0x140>(k2));
}
DI u32x4 mksrd(const void* p, unsigned bytes) { u64 a = (u64)p; u32x4 r = {(unsigned)a, (unsigned)(a >> 32) & 0xffffu, bytes, 0x00020000u}; return r; }
DI void dma(unsigned lds, unsigned voff, u32x4 srd, unsigned soff) {
  asm volatile("s_mov_b32 m0, %0\n\ts_nop 0\n\tbuffer_load_dwordx4 %1, %2, %3 offen lds" :: "s"(lds), "v"(voff), "s"(srd), "s"(soff) : "memory");
}
template<int I> DI void dmai(unsigned lds, unsigned voff, u32x4 srd, unsigned soff) {
  asm volatile("s_mov_b32 m0, %0\n\ts_nop 0\n\tbuffer_load_dwordx4 %1, %2, %3 offen offset:%4 lds" :: "s"(lds), "v"(voff), "s"(srd), "s"(soff), "n"(I * 1024) : "memory");
}
DI void dma4(unsigned lds, unsigned v0, unsigned v1, unsigned v2, unsigned v3, u32x4 srd, unsigned soff) {
  asm volatile("s_mov_b32 m0, %0\n\ts_nop 0\n\tbuffer_load_dwordx4 %1, %5, %6 offen lds\n\tbuffer_load_dwordx4 %2, %5, %6 offen offset:1024 lds\n\t"
               "buffer_load_dwordx4 %3, %5, %6 offen offset:2048 lds\n\tbuffer_load_dwordx4 %4, %5, %6 offen offset:3072 lds"
               :: "s"(lds), "v"(v0), "v"(v1), "v"(v2), "v"(v3), "s"(srd), "s"(soff) : "memory");
}
DI void dma2(unsigned lds, unsigned v0, unsigned v1, u32x4 srd, unsigned soff) {
  asm volatile("s_mov_b32 m0, %0\n\ts_nop 0\n\tbuffer_load_dwordx4 %1, %3, %4 offen lds\n\tbuffer_load_dwordx4 %2, %3, %4 offen offset:1024 lds"
               :: "s"(lds), "v"(v0), "v"(v1), "s"(srd), "s"(soff) : "memory");
}
DI void dma2_sc1(unsigned lds, unsigned v0, unsigned v1, u32x4 srd, unsigned soff) {
  asm volatile("s_mov_b32 m0, %0\n\ts_nop 0\n\tbuffer_load_dwordx4 %1, %3, %4 offen sc1 lds\n\tbuffer_load_dwordx4 %2, %3, %4 offen offset:1024 sc1 lds"
               :: "s"(lds), "v"(v0), "v"(v1), "s"(srd), "s"(soff) : "memory");
}
DI void touch(unsigned lds0, const void* a) {
  asm volatile("s_mov_b32 m0, %0\n\ts_nop 0\n\tglobal_load_lds_dword %1, off" :: "s"(lds0 + L_JUNK), "v"(a) : "memory");
}
DI void dma4_nt(unsigned lds, unsigned v0, unsigned v1, unsigned v2, unsigned v3, u32x4 srd, unsigned soff) {
  asm volatile("s_mov_b32 m0, %0\n\ts_nop 0\n\tbuffer_load_dwordx4 %1, %5, %6 offen nt lds\n\tbuffer_load_dwordx4 %2, %5, %6 offen offset:1024 nt lds\n\t"
               "buffer_load_dwordx4 %3, %5, %6 offen offset:2048 nt lds\n\tbuffer_load_dwordx4 %4, %5, %6 offen offset:3072 nt lds"
               :: "s"(lds), "v"(v0), "v"(v1), "v"(v2), "v"(v3), "s"(srd), "s"(soff) : "memory");
}
DI void dma2_sc1_nt(unsigned lds, unsigned v0, unsigned v1, u32x4 srd, unsigned soff) {
  asm volatile("s_mov_b32 m0, %0\n\ts_nop 0\n\tbuffer_load_dwordx4 %1, %3, %4 offen sc1 nt lds\n\tbuffer_load_dwordx4 %2, %3, %4 offen offset:1024 sc1 nt lds"
               :: "s"(lds), "v"(v0), "v"(v1), "s"(srd), "s"(soff) : "memory");
}
DI int wpos(int i) { return (int)((0x1980765432FEDCBAull >> (4 * i)) & 15); }
#define MFMA(acc, a, b) asm volatile("v_mfma_f32_16x16x32_bf16 %0, %1, %2, %0" : "+v"(acc) : "v"(a), "v"(b))
#define DSR(dst, addr, off) asm volatile("ds_read_b128 %0, %1 offset:%2" : "=v"(dst) : "v"(addr), "n"(off) : "memory")
#define ST128(v, off) __builtin_amdgcn_raw_buffer_store_b128(v, rws, off, 0, 16)
#define LD128(voff, soff) __builtin_amdgcn_raw_buffer_load_b128(rws, voff, soff, 16)
#define SPIN _Pragma("unroll 1") for (int spin = 0; spin < SPIN_CAP; ++spin)
typedef short v4i16_t __attribute__((ext_vector_type(4)));
DI s16x4 trd(const char* p) { return __builtin_amdgcn_ds_read_tr16_b64_v4i16((LAS v4i16_t*)p); }
DI bf16x8 cat(s16x4 a, s16x4 b) { return __builtin_shufflevector(a, b, 0, 1, 2, 3, 4, 5, 6, 7); }

#define ST(i)

__global__ __launch_bounds__(512, 2) void fused(const u16* __restrict__ x, const u16* __restrict__ Wre, const u16* __restrict__ Wim,
                                                const u16* __restrict__ Dre, const u16* __restrict__ Dim, const u16* __restrict__ wr,
                                                const u16* __restrict__ wg, const u16* __restrict__ wu, const u16* __restrict__ wd,
                                                const u16* __restrict__ cvec, const u16* __restrict__ wmod, u16* __restrict__ out, char* __restrict__ ws) {
  __shared__ __attribute__((aligned(16))) char smem[LDS_BYTES];
  ST(0);
  const u64 t_start = __builtin_amdgcn_s_memrealtime();
  const int tid = threadIdx.x, l = tid & 63;
  const int w = __builtin_amdgcn_readfirstlane(tid >> 6), lw = w & 3;
  const int b = blockIdx.x, g = b & 7, idx = b >> 3, pl = idx >> 3, j = idx & 7, p = 4 * g + pl;
  const unsigned lds0 = (unsigned)(size_t)smem;
  if (w == 4) {
    u64 pc; unsigned len;
    asm volatile("s_getpc_b64 %0\nfused_pc_mark:\n\ts_mov_b32 %1, fused_code_end - fused_pc_mark" : "=s"(pc), "=s"(len));
    dma(lds0 + L_JUNK, l * 16, mksrd((const void*)(pc & ~127ull), len & ~15u), idx * 1024);
  }
  u32x4 xv;
  asm volatile("global_load_dwordx4 %0, %1, %2" XNT : "=v"(xv) : "v"(l * 16), "s"(x + (size_t)(8 * b + w) * 512) : "memory");
  const __amdgpu_buffer_rsrc_t rws = __builtin_amdgcn_make_buffer_rsrc(ws, 0, WS_END, 0x00020000);
#define PLROT 2
  const int kt0 = 4 * ((j + PLROT * pl) & 7);
  const int ncol = 3 + (g & 1), col0 = (7 * g) >> 1;
  const bool is_prod = j >= 4 && pl < ncol, is_red = j == 1 || j == 2;
  const u32x4 srdA = mksrd((lw < 2 ? Wre : Wim) + (size_t)(64 * p + 32 * (lw & 1)) * 2048, 32 * 4096);
  const unsigned vA0 = (l >> 3) * 4096 + (((l & 7) ^ (l >> 4)) << 4), vA1 = (vA0 ^ 64) + 8 * 4096 - 1024, vA2 = vA0 + 16 * 4096 - 2048, vA3 = vA1 + 16 * 4096 - 2048;
  const unsigned ldsA = lds0 + L_A + lw * 4096;
#define PIECE_A(c) dmai<((c) >> 2)>(ldsA + ((c) & 3) * 16384, ((c) >> 2) == 0 ? vA0 : ((c) >> 2) == 1 ? vA1 : ((c) >> 2) == 2 ? vA2 : vA3, srdA, (kt0 + ((c) & 3)) * 128)
#define PIECES_A(lo, hi) do { if (lo <= 0 && 0 < hi) PIECE_A(0); if (lo <= 1 && 1 < hi) PIECE_A(1); if (lo <= 2 && 2 < hi) PIECE_A(2); if (lo <= 3 && 3 < hi) PIECE_A(3); \
    if (lo <= 4 && 4 < hi) PIECE_A(4); if (lo <= 5 && 5 < hi) PIECE_A(5); if (lo <= 6 && 6 < hi) PIECE_A(6); if (lo <= 7 && 7 < hi) PIECE_A(7); \
    if (lo <= 8 && 8 < hi) PIECE_A(8); if (lo <= 9 && 9 < hi) PIECE_A(9); if (lo <= 10 && 10 < hi) PIECE_A(10); if (lo <= 11 && 11 < hi) PIECE_A(11); \
    if (lo <= 12 && 12 < hi) PIECE_A(12); if (lo <= 13 && 13 < hi) PIECE_A(13); if (lo <= 14 && 14 < hi) PIECE_A(14); if (lo <= 15 && 15 < hi) PIECE_A(15); } while (0)
  const bool do_mod = idx < 24;
  const int mu = g * 24 + idx, kc = mu / 48, cc = mu - 48 * kc;
  u32x4 wv[4]; u32x2 cv; unsigned ep, wrv;
  int role = w; asm volatile("" : "+s"(role));
  if (role >= 4) {
    __builtin_amdgcn_s_setprio(LPRIO);
    PIECES_A(0, NB1);
    wait_vm<NB1>();
  } else {
    const u32x4 rwm = mksrd(wmod + (kc * 128 + 32 * w) * 3072 + cc * 64, do_mod ? 32 * 6144 : 0), rcv = mksrd(cvec + kc * 128 + 32 * w, do_mod ? 64 : 0);
    const unsigned vm = (l >> 3) * 4 * 6144 + (l & 7) * 16;
    asm volatile("s_nop 4\n\tglobal_load_dword %0, %7, %8 sc1\n\tbuffer_load_dwordx2 %1, %9, %10, 0 offen\n\tglobal_load_dword %2, %11, %12\n\t"
                 "buffer_load_dwordx4 %3, %13, %14, 0 offen" XNT "\n\tbuffer_load_dwordx4 %4, %13, %14, %15 offen" XNT "\n\t"
                 "buffer_load_dwordx4 %5, %13, %14, %16 offen" XNT "\n\tbuffer_load_dwordx4 %6, %13, %14, %17 offen" XNT
                 : "=&v"(ep), "=&v"(cv), "=&v"(wrv), "=&v"(wv[0]), "=&v"(wv[1]), "=&v"(wv[2]), "=&v"(wv[3])
                 : "v"(0), "s"(ws + WS_EPOCH), "v"((l >> 3) * 8), "s"(rcv), "v"((l & 31) * 4), "s"(wr + 64 * j), "v"(vm), "s"(rwm), "s"(6144), "s"(2 * 6144), "s"(3 * 6144) : "memory");
    if (tid < 16) *(unsigned*)(smem + L_SEL + tid * 4) = 0u;
    wait_vm<7>();
  }
  asm volatile("" : "+v"(xv) :: "memory");
  ST(1);
  {
    f32x2 a[4], s1 = {0.f, 0.f}, s2 = {0.f, 0.f};
#pragma unroll
    for (int i = 0; i < 4; ++i) { a[i] = f32x2{lo16(xv[i]), hi16(xv[i])}; s1 += a[i]; s2 += a[i] * a[i]; }
    const auto r0 = __builtin_amdgcn_permlane32_swap(asu(s1[0] + s1[1]), asu(s2[0] + s2[1]), false, false);
    const float r1 = sum_x16(sum_row(asf(r0[0]) + asf(r0[1]))) * (1.f / 512);
    const float m = asf(__builtin_amdgcn_readlane(asu(r1), 0)), e2 = asf(__builtin_amdgcn_readlane(asu(r1), 32));
    const float rs = __builtin_amdgcn_rsqf(fmaxf(e2 - m * m, 0.f) + 1e-5f);
    u32x4 o;
#pragma unroll
    for (int i = 0; i < 4; ++i) { const f32x2 n = (a[i] - m) * rs; o[i] = pk2(n[0], n[1]); }
    *(u32x4*)(smem + L_T + w * 1088 + l * 16) = o;
  }
  barl();
  if (w >= 4) {
    __builtin_amdgcn_s_sleep(DLY_L);
    PIECES_A(NB1, NB1 + NB2);
    lds_wait(smem + L_CNT + 4, 1);
    PIECES_A(NB1 + NB2, 16);
    if (lw == 0) touch(lds0, (const char*)(b < 128 ? Dre : Dim) + (b & 127) * 4096 + l * 64);
    const unsigned tag = __builtin_amdgcn_readfirstlane(*(const unsigned*)(smem + L_TAG));
    unsigned vW0 = (l >> 3) * 1024 + (((l & 7) ^ (l >> 4)) << 4), vW1 = (vW0 ^ 64) + 8 * 1024 - 1024;
    u32x4 srdQ[4], srdX = mksrd(x + (size_t)(64 * p + 16 * lw) * 512 + 64 * j, 16 * 1024);
#pragma unroll
    for (int z = 0; z < 4; ++z) srdQ[z] = mksrd((((z + pl) & 2) ? Dim : Dre) + (size_t)(64 * j + 16 * lw) * 512 + ((z + pl) & 1) * 256, 16 * 1024 - 512);
    const unsigned dW = lds0 + L_W + lw * 2048;
    auto issueW = [&](int i) { dma2(dW + wpos(i) * 8192, vW0, vW1, srdQ[i >> 2], (i & 3) * 128); };
    const char* ta[2];
#pragma unroll
    for (int z = 0; z < 2; ++z) {
      const int tix = ((2 * lw + z) * 32 + idx) * 32 + (l >> 1), n1 = 1024 * ncol;
      const u16* a;
      if (tix < n1) { const int cix = tix >> 10, r = tix & 1023; a = (r < 512 ? wg : wu) + (size_t)(r & 511) * 1792 + (col0 + cix) * 64; }
      else { const int y = min(tix - n1, 512 * ncol - 1), cix = y >> 9; a = wd + (size_t)((col0 + cix) * 64 + ((y & 511) >> 3)) * 512 + (y & 7) * 64; }
      ta[z] = (const char*)a + (l & 1) * 64;
    }
    asm volatile("" : "+v"(vW0), "+v"(vW1), "+v"(ta[0]), "+v"(ta[1]));
    asm volatile("" : "+s"(srdQ[0]), "+s"(srdQ[1]), "+s"(srdQ[2]), "+s"(srdQ[3]), "+s"(srdX));
    ST(12);
    wait_vm<0>();
    ST(13);
#pragma unroll
    for (int s = 4; s < NA; ++s) dma4(ldsA + s * 16384, vA0, vA1, vA2, vA3, srdA, ((kt0 + s) & 31) * 128);
    ST(2);
    const u32x4 srdB = mksrd(ws + WS_N8 + j * 262144 + lw * 2048, 262144 - lw * 2048);
    const unsigned vB0 = l * 16, vB1 = l * 16;
    unsigned soB = kt0 << 13, dB = 0, soA = ((kt0 + NA) & 31) * 128, dA = ldsA;
    auto nextB = [&]() { soB = (soB + 8192) & 0x3ffff; dB = (dB + 8192) & 0x7fff; };
    auto nextA = [&]() { soA = (soA + 128) & 0xfff; dA = dA == ldsA + (NA - 1) * 16384 ? ldsA : dA + 16384; };
    auto issueB = [&]() { dma2_sc1(lds0 + L_B + lw * 2048 + dB, vB0, vB1, srdB, soB); nextB(); };
    auto stageA = [&]() { dma4(dA, vA0, vA1, vA2, vA3, srdA, soA); nextA(); };
    auto issueBl = [&]() { if (LASTNT & 1) dma2_sc1_nt(lds0 + L_B + lw * 2048 + dB, vB0, vB1, srdB, soB); else dma2_sc1(lds0 + L_B + lw * 2048 + dB, vB0, vB1, srdB, soB); nextB(); };
    auto stageAl = [&]() { if (LASTNT & 2) dma4_nt(dA, vA0, vA1, vA2, vA3, srdA, soA); else dma4(dA, vA0, vA1, vA2, vA3, srdA, soA); nextA(); };
    bar();
    ST(3);
    issueB(); issueB(); issueB();
    wait_vm<4>();
#pragma unroll 1
    for (int z = 0; z < 3; ++z) {
      bar(); issueB(); if (z == 0) wait_vm<4>(); else wait_vm<8>();
      bar(); stageA(); issueB(); wait_vm<8>();
#pragma unroll 1
      for (int kt = 0; kt < 5; ++kt) { bar(); stageA(); issueB(); wait_vm<12>(); }
    }
    bar(); issueBl(); wait_vm<8>();
    bar(); stageA(); issueBl(); wait_vm<8>();
#pragma unroll 1
    for (int kt = 23; kt < 25; ++kt) { bar(); stageA(); issueBl(); wait_vm<12>(); }
#pragma unroll 1
    for (int kt = 25; kt < 29; ++kt) { bar(); stageAl(); issueBl(); wait_vm<12>(); }
    {
      bar();
#pragma unroll
      for (int i = 0; i < W29; ++i) issueW(i);
      wait_vm<6 + 2 * W29>();
      bar();
#pragma unroll
      for (int i = W29; i < W30; ++i) issueW(i);
      wait_vm<2 * W30>();
      bar();
      dma2(lds0 + L_X + lw * 2048, vW0, vW1, srdX, 0);
#pragma unroll
      for (int i = W30; i < W31; ++i) issueW(i);
      ST(4);
      lds_wait(smem + L_CNT + 24, 256);
      touch(lds0, ta[0]); touch(lds0, ta[1]);
#pragma unroll
      for (int i = W31; i < 16 - WTAIL; ++i) issueW(i);
      lds_wait(smem + L_CNT + 8, 256);
      __builtin_amdgcn_s_sleep(DLY_P);
      ST(5);
      const unsigned fo = WS_FLAGP + (g + 8 * (8 * pl + (l & 7))) * 128;
      unsigned f;
      if (lw == 3) asm volatile("buffer_load_dword %0, %1, %2, 0 offen sc1" : "=v"(f) : "v"(fo), "s"(mksrd(ws, WS_END)) : "memory");
#pragma unroll
      for (int i = 16 - WTAIL; i < 16; ++i) issueW(i);
      if (lw == 3) {
        asm volatile("s_waitcnt vmcnt(%1)" : "+v"(f) : "n"(2 * WTAIL) : "memory");
        if (!__all(f == tag)) {
          SPIN {
            asm volatile("" ::: "memory");
            if (__all(__builtin_amdgcn_raw_buffer_load_b32(rws, fo, 0, 16) == tag)) break;
            __builtin_amdgcn_s_sleep(1);
          }
        }
        *(unsigned*)(smem + L_CNT + 20) = 1u;
      }
    }
    asm volatile("s_waitcnt vmcnt(0) lgkmcnt(0)" ::: "memory");
    bar();
    bf16x8 fg[4], fu[4]; s16x4 fd[8];
    const int fs = (col0 + pl) * 4 + (j & 3);
    const int q = l >> 4, q4 = (l & 15) >> 2, p4 = l & 3;
    u32x4 rg[4], ru[4], rd[4];
    const __amdgpu_buffer_rsrc_t rwg = __builtin_amdgcn_make_buffer_rsrc((void*)(wg + 128 * lw * 1792 + 16 * fs), 0, is_prod ? 128 * 3584 - 32 * fs : 0, 0x00020000);
    const __amdgpu_buffer_rsrc_t rwu = __builtin_amdgcn_make_buffer_rsrc((void*)(wu + 128 * lw * 1792 + 16 * fs), 0, is_prod ? 128 * 3584 - 32 * fs : 0, 0x00020000);
    const __amdgpu_buffer_rsrc_t rwd = __builtin_amdgcn_make_buffer_rsrc((void*)(wd + 16 * fs * 512 + 128 * lw), 0, is_prod ? 16 * 1024 - 256 * lw : 0, 0x00020000);
    const unsigned vg = (l >> 1) * 3584 + (l & 1) * 16;
    if (is_prod) {
      lds_wait<2>(smem + L_CNT + 28, 256);
#pragma unroll
      for (int i = 0; i < 4; ++i) {
        if (i < G_EARLY) rg[i] = __builtin_amdgcn_raw_buffer_load_b128(rwg, vg, i * 32 * 3584, 0);
        rd[i] = __builtin_amdgcn_raw_buffer_load_b128(rwd, q * 1024 + (l & 15) * 16, i * 4096, 0);
        if (i < UP_EARLY) ru[i] = __builtin_amdgcn_raw_buffer_load_b128(rwu, vg, i * 32 * 3584, 0);
      }
    }
    bar();
    if (is_prod) {
#pragma unroll
      for (int i = G_EARLY; i < 4; ++i) rg[i] = __builtin_amdgcn_raw_buffer_load_b128(rwg, vg, i * 32 * 3584, 0);
#pragma unroll
      for (int i = UP_EARLY; i < 4; ++i) ru[i] = __builtin_amdgcn_raw_buffer_load_b128(rwu, vg, i * 32 * 3584, 0);
      char* G = smem + L_W + lw * 12288;
#pragma unroll
      for (int i = 0; i < 4; ++i) { *(u32x4*)(G + i * 1024 + l * 16) = rg[i]; *(u32x4*)(G + 8192 + i * 1024 + l * 16) = rd[i]; }
#pragma unroll
      for (int i = 0; i < 4; ++i) *(u32x4*)(G + 4096 + i * 1024 + l * 16) = ru[i];
      wait_lgkm();
#pragma unroll
      for (int i = 0; i < 4; ++i) { const char* a = G + (32 * i + 8 * q + q4) * 32 + p4 * 8; fg[i] = cat(trd(a), trd(a + 128)); fu[i] = cat(trd(a + 4096), trd(a + 4096 + 128)); }
#pragma unroll
      for (int i = 0; i < 8; ++i) fd[i] = trd(G + 8192 + (4 * q + q4) * 256 + (16 * i + 4 * p4) * 2);
    }
    ST(6);
    unsigned k1, k2;
    if (lw == 0) {
      lds_wait(smem + L_CNT + 16, 1);
      ST(7);
      if (is_prod || is_red) __builtin_amdgcn_s_sleep(DLY_C); else __builtin_amdgcn_s_sleep(DLY_LAZY);
      u32x4 v0 = {}, v1 = {};
      v0[1] = v1[1] = ~tag;
      SPIN {
        asm volatile("" ::: "memory");
        if (TAG2(v0) | TAG2(v1)) { v0 = LD128(WS_CANDG + l * 128, 0); v1 = LD128(WS_CANDG + l * 128, 64 * 128); }
        if (none(TAG2(v0) | TAG2(v1))) break;
        __builtin_amdgcn_s_sleep(1);
      }
      k1 = v0[0]; k2 = v0[2]; top2(k1, k2, v1[0], v1[2]);
      top2_row(k1, k2);
      { unsigned a1 = __builtin_amdgcn_readlane(k1, 0), a2 = __builtin_amdgcn_readlane(k2, 0), b1 = __builtin_amdgcn_readlane(k1, 16), b2 = __builtin_amdgcn_readlane(k2, 16);
        unsigned c1 = __builtin_amdgcn_readlane(k1, 32), c2 = __builtin_amdgcn_readlane(k2, 32), d1 = __builtin_amdgcn_readlane(k1, 48), d2 = __builtin_amdgcn_readlane(k2, 48);
        top2(a1, a2, b1, b2); top2(c1, c2, d1, d2); top2(a1, a2, c1, c2); k1 = a1; k2 = a2; }
      *(u32x4*)(smem + L_SEL) = u32x4{k1, k2, 1u, 0u};
    } else {
      u32x4 z = {};
      if (is_prod) {
#pragma unroll 1
        for (int spin = 0; spin < (SPIN_CAP << 6); ++spin) {
          asm volatile("" ::: "memory");
          z = *(const u32x4*)(smem + L_SEL);
          if (__builtin_amdgcn_readfirstlane(z[2]) == 1) break;
        }
      }
      k1 = __builtin_amdgcn_readfirstlane(z[0]); k2 = __builtin_amdgcn_readfirstlane(z[1]);
    }
    ST(8);
    const int r1 = 2047 - (k1 & 2047), r2 = 2047 - (k2 & 2047);
    const unsigned slot1 = (r1 >> 4) * 2, slot2 = (r2 >> 4) * 2 + ((r1 >> 4) == (r2 >> 4));
    if (is_prod) {
      const unsigned ho = WS_ROWG + (l < 32 ? slot1 : slot2) * 8192 + lw * 512 + (l & 31) * 16;
      u32x4 v;
      SPIN {
        asm volatile("" ::: "memory");
        v = LD128(ho, 0);
        if (none(TAG2(v))) break;
        __builtin_amdgcn_s_sleep(1);
      }
      ST(9);
      char* H = smem + L_W + 49152 + lw * 512;
      *(u32x2*)(H + l * 8) = u32x2{v[0], v[2]};
      wait_lgkm();
      f32x4 ag = {}, au = {};
#pragma unroll
      for (int i = 0; i < 4; ++i) {
        const bf16x8 h = *(const bf16x8*)(H + (l & 1) * 256 + i * 64 + q * 16);
        ag = __builtin_amdgcn_mfma_f32_16x16x32_bf16(fg[i], h, ag, 0, 0, 0);
        au = __builtin_amdgcn_mfma_f32_16x16x32_bf16(fu[i], h, au, 0, 0, 0);
      }
      if ((l & 15) < 4) *(f32x4*)(smem + L_R + lw * 256 + (l & 3) * 64 + q * 16) = (l & 2) ? au : ag;
    }
    barl();
    ST(10);
    if (is_prod) {
      f32x4 sg = {}, su = {};
#pragma unroll
      for (int i = 0; i < 4; ++i) { const char* R = smem + L_R + i * 256 + (l & 1) * 64 + q * 16; sg += *(const f32x4*)R; su += *(const f32x4*)(R + 128); }
      const s16x4 af = __builtin_bit_cast(s16x4, (u32x2{pk2(silu(sg[0]) * su[0], silu(sg[1]) * su[1]), pk2(silu(sg[2]) * su[2], silu(sg[3]) * su[3])}));
#pragma unroll
      for (int i = 0; i < 2; ++i) {
        f32x4 y[4];
#pragma unroll
        for (int z = 0; z < 4; ++z) y[z] = __builtin_amdgcn_mfma_f32_16x16x16bf16_1k(af, fd[4 * i + z], (f32x4{0.f, 0.f, 0.f, 0.f}), 0, 0, 0);
        const f32x4 s = q == 0 ? y[0] : q == 1 ? y[1] : q == 2 ? y[2] : y[3];
        ST128((u32x4{asu(s[0]), tag, asu(s[1]), tag}), WS_YG + fs * 8192 + (128 * lw + 64 * i + l) * 16);
      }
    }
    if (is_red) {
      const u32x2 sel = *(const u32x2*)(smem + L_SEL);
      const int m = (l >> 3) & 1, e = 16 * p + 8 * (j - 1) + (l & 7);
      const unsigned km = m ? sel[1] : sel[0];
      const int q1 = 2047 - (sel[0] & 2047), q2 = 2047 - (sel[1] & 2047);
      const unsigned sl = m ? (q2 >> 4) * 2 + ((q1 >> 4) == (q2 >> 4)) : (q1 >> 4) * 2;
      float base = 0.f, scale = 0.f;
      if (lw == 0) {
        u32x2 og;
        SPIN {
          asm volatile("" ::: "memory");
          og = __builtin_amdgcn_raw_buffer_load_b64(rws, WS_ROWG + sl * 8192 + 2048 + e * 8, 0, 16);
          if (__all(og[1] == tag)) break;
          __builtin_amdgcn_s_sleep(1);
        }
        const unsigned ku = km & ~2047u;
        const float rv = asf((ku & 0x80000000u) ? (ku & 0x7fffffffu) : ~(ku | 2047u));
        base = lo16(og[0]);
        scale = hi16(og[0]) * __builtin_amdgcn_rcpf(1.f + __expf(-rv));
        __builtin_amdgcn_s_sleep(DLY_Y - 12);
      } else {
        __builtin_amdgcn_s_sleep(DLY_Y);
      }
      float y0, y1;
      const unsigned vo = WS_YG + ((l >> 3) + 28 * lw) * 8192 + e * 16, vo3 = vo + (l < 32 ? 24 : 20) * 8192;
      SPIN {
        asm volatile("" ::: "memory");
        unsigned bad = 0; y0 = 0.f; y1 = 0.f;
#pragma unroll
        for (int i = 0; i < 4; ++i) {
          const u32x4 v = i < 3 ? LD128(vo, i * 8 * 8192) : LD128(vo3, 0);
          const bool use = i < 3 || l < 32;
          bad |= TAG2(v); y0 += use ? asf(v[0]) : 0.f; y1 += use ? asf(v[2]) : 0.f;
        }
        if (none(bad)) break;
        __builtin_amdgcn_s_sleep(1);
      }
      ST(11);
      y0 += dppf<0x128>(y0); y1 += dppf<0x128>(y1);
      const float y = sum_q(m ? y1 : y0);
      if (l < 16) *(float*)(smem + L_R + l * 16 + lw * 4) = y;
      barl();
      if (lw == 0 && l < 16) {
        const f32x4 R = *(const f32x4*)(smem + L_R + l * 16);
        const float o = base + scale * ((R[0] + R[1]) + (R[2] + R[3]));
        out[(size_t)(m ? q2 : q1) * 512 + e] = (u16)pk2(o, o);
      }
    }
    asm volatile("fused_code_end:" ::: "memory");
  } else {
    const int t = l & 15, q = l >> 4;
    unsigned tag;
    {
#pragma unroll
      for (int h = 0; h < 2; ++h) {
        const char* a = smem + L_T + (t >> 2) * 1088 + (256 * h + 64 * w + 16 * q + 4 * (t & 3)) * 2;
        ST128(__builtin_bit_cast(u32x4, cat(trd(a), trd(a + 4 * 1088))), WS_N8 + (w + 4 * h) * 262144 + b * 1024 + l * 16);
      }
      ST(2);
      fence();
      asm volatile("s_waitcnt vmcnt(2)" : "+v"(ep), "+v"(wrv), "+v"(cv), "+v"(wv[0]), "+v"(wv[1]), "+v"(wv[2]), "+v"(wv[3]) :: "memory");
      tag = __builtin_amdgcn_readfirstlane(ep) + 1u;
      if (tid == 0) *(unsigned*)(smem + L_TAG) = tag;
      if (w == 3) *(f32x2*)(smem + L_MOD + 6 * 256 + (l & 31) * 8) = f32x2{lo16(wrv), hi16(wrv)};
      if (do_mod) {
        f32x2 acc[4] = {};
#pragma unroll
        for (int i = 0; i < 4; ++i) {
          const float s = bfr(silu((i & 1) ? hi16(cv[i >> 1]) : lo16(cv[i >> 1])));
#pragma unroll
          for (int z = 0; z < 4; ++z) acc[z] += s * f32x2{lo16(wv[i][z]), hi16(wv[i][z])};
        }
#pragma unroll
        for (int z = 0; z < 4; ++z) acc[z] += f32x2{dppf<0x128>(acc[z][0]), dppf<0x128>(acc[z][1])};
        if (t < 8) {
          f32x4* M = (f32x4*)(smem + L_M + (w * 4 + q) * 256 + t * 32);
          M[0] = f32x4{acc[0][0], acc[0][1], acc[1][0], acc[1][1]}; M[1] = f32x4{acc[2][0], acc[2][1], acc[3][0], acc[3][1]};
        }
      }
      asm volatile("s_waitcnt vmcnt(0) lgkmcnt(0)" ::: "memory");
      if (lds_arrive(smem + L_CNT) == 192) {
        if (l == 0) { __builtin_amdgcn_raw_buffer_store_b32(tag, rws, WS_FLAGN + (b & 63) * 128 + (b >> 6) * 4, 0, 16); }
        if (do_mod) {
          float s = 0.f;
#pragma unroll
          for (int i = 0; i < 16; ++i) s += *(const float*)(smem + L_M + i * 256 + l * 4);
          __builtin_amdgcn_raw_buffer_store_b64(u32x2{asu(s), tag}, rws, WS_MODG + (kc * 3072 + cc * 64 + l) * 8, 0, 16);
        }
        asm volatile("" ::: "memory");
        *(unsigned*)(smem + L_CNT + 4) = 1u;
      }
      ST(3);
      while (__builtin_amdgcn_s_memrealtime() - t_start < T_POLL) __builtin_amdgcn_s_sleep(1);
      const int which = tid >> 5;
      u32x4 m[4];
      if (w < 3) {
        const unsigned mo = WS_MODG + (unsigned)(which * 512 + 64 * j + 2 * (tid & 31)) * 8;
        SPIN {
          asm volatile("" ::: "memory");
          unsigned bad = 0;
#pragma unroll
          for (int i = 0; i < 4; ++i) { m[i] = LD128(mo, i * 3072 * 8); bad |= TAG2(m[i]); }
          if (none(bad)) break;
          __builtin_amdgcn_s_sleep(2);
        }
      } else {
        SPIN {
          asm volatile("" ::: "memory");
          const u32x4 f = LD128(WS_FLAGN + l * 128, 0);
          if (none((f[0] ^ tag) | (f[2] ^ tag) | TAG2(f))) break;
          __builtin_amdgcn_s_sleep(2);
        }
      }
      ST(4);
      bar();
      if (w < 3) {
        const float one = (which == 1 || which == 4) ? 1.f : 0.f;
        const float v0 = bfr(one + bfr((asf(m[0][0]) + asf(m[1][0])) + (asf(m[2][0]) + asf(m[3][0]))));
        const float v1 = bfr(one + bfr((asf(m[0][2]) + asf(m[1][2])) + (asf(m[2][2]) + asf(m[3][2]))));
        *(f32x2*)(smem + L_MOD + which * 256 + (tid & 31) * 8) = f32x2{v0, v1};
      } else if (b == 0 && l == 0) {
        __builtin_amdgcn_raw_buffer_store_b32(tag, rws, 0, WS_EPOCH, 16);
      }
      wait_lgkm();
    }
    {
      f32x4 acc[2][4] = {};
      const unsigned ao = lds0 + L_A + w * 4096 + t * 128 + ((q ^ (t >> 1)) << 4), bo = lds0 + L_B + q * 1024 + t * 16;
      bf16x8 af[2][2][2], bf[2][2][4];
      {
        const bf16x8 ones = {0x3f80, 0x3f80, 0x3f80, 0x3f80, 0x3f80, 0x3f80, 0x3f80, 0x3f80};
        bf16x8 u[16];
#pragma unroll
        for (int z = 0; z < 16; ++z) u[z] = *(const bf16x8*)(smem + ((ao - lds0) ^ ((z & 1) << 6)) + (z >> 2) * 16384 + ((z >> 1) & 1) * 2048);
        fence();
        f32x4 r[2] = {};
#pragma unroll
        for (int z = 0; z < 16; ++z) r[(z >> 1) & 1] = __builtin_amdgcn_mfma_f32_16x16x32_bf16(ones, u[z], r[(z >> 1) & 1], 0, 0, 0);
        if (l < 16) ST128((u32x4{asu(r[0][0]), tag, asu(r[1][0]), tag}), WS_RSG + (b * 4 + w) * 256 + l * 16);
      }
      const unsigned ro = WS_RSG + ((g + 64 * pl + 16 * q) * 4 + w) * 256 + t * 16;
      u32x4 rg0 = {}, rg1 = {};
      unsigned sa = 0, sb = 0, pa0, pa1, pb;
#define G1_ADDR() do { pa0 = ao + sa; pa1 = (ao ^ 64) + sa; pb = bo + sb; sa = sa == (NA - 1) * 16384 ? 0 : sa + 16384; sb = (sb + 8192) & 0x7fff; } while (0)
#define G1_SYNC(z) asm volatile("s_waitcnt lgkmcnt(0)\n\ts_barrier" : "+v"(af[z][0][0]), "+v"(af[z][0][1]), "+v"(af[z][1][0]), "+v"(af[z][1][1]), \
        "+v"(bf[z][0][0]), "+v"(bf[z][0][1]), "+v"(bf[z][0][2]), "+v"(bf[z][0][3]), "+v"(bf[z][1][0]), "+v"(bf[z][1][1]), "+v"(bf[z][1][2]), "+v"(bf[z][1][3]) :: "memory")
#define G1_STEP(z) do { \
        MFMA(acc[0][0], bf[z][0][0], af[z][0][0]); DSR(af[1 - z][0][0], pa0, 0);     MFMA(acc[0][1], bf[z][0][1], af[z][0][0]); DSR(bf[1 - z][0][0], pb, 0); \
        MFMA(acc[0][2], bf[z][0][2], af[z][0][0]); DSR(bf[1 - z][0][1], pb, 256);    MFMA(acc[0][3], bf[z][0][3], af[z][0][0]); DSR(bf[1 - z][0][2], pb, 512); \
        MFMA(acc[1][0], bf[z][0][0], af[z][0][1]); DSR(bf[1 - z][0][3], pb, 768);    MFMA(acc[1][1], bf[z][0][1], af[z][0][1]); DSR(af[1 - z][0][1], pa0, 2048); \
        MFMA(acc[1][2], bf[z][0][2], af[z][0][1]); DSR(af[1 - z][1][0], pa1, 0);     MFMA(acc[1][3], bf[z][0][3], af[z][0][1]); DSR(bf[1 - z][1][0], pb, 4096); \
        MFMA(acc[0][0], bf[z][1][0], af[z][1][0]); DSR(bf[1 - z][1][1], pb, 4352);   MFMA(acc[0][1], bf[z][1][1], af[z][1][0]); DSR(bf[1 - z][1][2], pb, 4608); \
        MFMA(acc[0][2], bf[z][1][2], af[z][1][0]); DSR(bf[1 - z][1][3], pb, 4864);   MFMA(acc[0][3], bf[z][1][3], af[z][1][0]); DSR(af[1 - z][1][1], pa1, 2048); \
        MFMA(acc[1][0], bf[z][1][0], af[z][1][1]); fence(); G1_ADDR(); fence();      MFMA(acc[1][1], bf[z][1][1], af[z][1][1]); \
        MFMA(acc[1][2], bf[z][1][2], af[z][1][1]); MFMA(acc[1][3], bf[z][1][3], af[z][1][1]); } while (0)
      G1_ADDR();
      bar();
      DSR(af[0][0][0], pa0, 0); DSR(bf[0][0][0], pb, 0); DSR(bf[0][0][1], pb, 256); DSR(bf[0][0][2], pb, 512); DSR(bf[0][0][3], pb, 768); DSR(af[0][0][1], pa0, 2048);
      DSR(af[0][1][0], pa1, 0); DSR(bf[0][1][0], pb, 4096); DSR(bf[0][1][1], pb, 4352); DSR(bf[0][1][2], pb, 4608); DSR(bf[0][1][3], pb, 4864); DSR(af[0][1][1], pa1, 2048);
      fence(); G1_ADDR(); fence();
#pragma unroll 1
      for (int kt = 1; kt < 31; kt += 2) {
        G1_SYNC(0);
        G1_STEP(0);
        G1_SYNC(1); G1_STEP(1);
        if (kt == KT_RS) { rg0 = LD128(ro, 0); rg1 = LD128(ro, 8192); }
      }
      G1_SYNC(0);
      G1_STEP(0);
      asm volatile("s_waitcnt lgkmcnt(0)" : "+v"(af[1][0][0]), "+v"(af[1][0][1]), "+v"(af[1][1][0]), "+v"(af[1][1][1]), "+v"(bf[1][0][0]), "+v"(bf[1][0][1]),
                   "+v"(bf[1][0][2]), "+v"(bf[1][0][3]), "+v"(bf[1][1][0]), "+v"(bf[1][1][1]), "+v"(bf[1][1][2]), "+v"(bf[1][1][3]) :: "memory");
      f32x4 sa4[4], ca4[4];
#pragma unroll
      for (int db = 0; db < 4; ++db) {
        sa4[db] = *(const f32x4*)(smem + L_MOD + (16 * db + 4 * q) * 4);
        ca4[db] = *(const f32x4*)(smem + L_MOD + 256 + (16 * db + 4 * q) * 4);
      }
      fence();
#pragma unroll
      for (int tb = 0; tb < 2; ++tb)
#pragma unroll
        for (int kk = 0; kk < 2; ++kk)
#pragma unroll
          for (int db = 0; db < 4; ++db) MFMA(acc[tb][db], bf[1][kk][db], af[1][kk][tb]);
      fence();
      if (!none(TAG2(rg0) | TAG2(rg1))) {
        SPIN {
          asm volatile("" ::: "memory");
          rg0 = LD128(ro, 0); rg1 = LD128(ro, 8192);
          if (none(TAG2(rg0) | TAG2(rg1))) break;
          __builtin_amdgcn_s_sleep(1);
        }
      }
      const float rsum[2] = {sum_q(asf(rg0[0]) + asf(rg1[0])), sum_q(asf(rg0[2]) + asf(rg1[2]))};
      asm volatile("s_nop 7" : "+v"(acc[0][0]), "+v"(acc[0][1]), "+v"(acc[0][2]), "+v"(acc[0][3]), "+v"(acc[1][0]), "+v"(acc[1][1]), "+v"(acc[1][2]), "+v"(acc[1][3]));
      ST(5);
      const unsigned sgn = w < 2 ? 0u : 0x80008000u;
      const unsigned po = WS_PQ + ((p * 4 + 2 * (w & 1)) * 128 + 64 * (w >> 1) + 8 * j + 2 * (q & 1) + (q >> 1)) * 256 + t * 16;
#pragma unroll
      for (int tb = 0; tb < 2; ++tb)
#pragma unroll
        for (int db = 0; db < 4; db += 2) {
          unsigned pkd[4];
#pragma unroll
          for (int z = 0; z < 2; ++z) {
            const f32x4 o = acc[tb][db + z] * ca4[db + z] + rsum[tb] * sa4[db + z];
            pkd[2 * z] = pk2(o[0], o[1]) ^ sgn; pkd[2 * z + 1] = pk2(o[2], o[3]) ^ sgn;
          }
          const auto s0 = __builtin_amdgcn_permlane16_swap(pkd[0], pkd[2], false, false);
          const auto s1 = __builtin_amdgcn_permlane16_swap(pkd[1], pkd[3], false, false);
          ST128((u32x4{s0[0], s1[0], s0[1], s1[1]}), po + tb * 32768 + db * 512);
        }
    }
    (void)lds_arrive(smem + L_CNT + 24);
    wait_vm<0>();
    if (lds_arrive(smem + L_CNT + 8) == 192 && l == 0) __builtin_amdgcn_raw_buffer_store_b32(tag, rws, WS_FLAGP + b * 128, 0, 16);
    ST(6);
    f32x4 ga[4], uu[4]; float ucv;
    {
      f32x4 us = {}, cs = {};
#pragma unroll
      for (int eb = 0; eb < 4; ++eb) {
        const char* M = smem + L_MOD + (16 * eb + 4 * q) * 4;
        const f32x4 wr4 = *(const f32x4*)(M + 6 * 256);
        ga[eb] = *(const f32x4*)(M + 2 * 256); uu[eb] = *(const f32x4*)(M + 4 * 256) * wr4;
        us += uu[eb]; cs += *(const f32x4*)(M + 3 * 256) * wr4;
      }
      ucv = sum_q((t & 1) ? (cs[0] + cs[1]) + (cs[2] + cs[3]) : (us[0] + us[1]) + (us[2] + us[3]));
    }
    lds_wait(smem + L_CNT + 20, 1);
    ST(7);
    f32x4 x1[4];
    {
      f32x4 ac[4] = {};
      u32x4 pf[8];
      const unsigned pb = WS_PQ + (p * 4 + w) * 32768;
#pragma unroll
      for (int i = 0; i < 8; ++i) pf[i] = LD128(l * 16 + (i & 3) * 1024, pb + (((i >> 2) + j) & 7) * 4096);
      bar();
#pragma unroll
      for (int eb = 0; eb < 4; ++eb) {
        const u32x2 xq = *(const u32x2*)(smem + L_X + (16 * w + t) * 128 + (((2 * eb + (q >> 1)) ^ (t >> 1)) << 4) + (q & 1) * 8);
        x1[eb] = f32x4{lo16(xq[0]), hi16(xq[0]), lo16(xq[1]), hi16(xq[1])};
      }
      const int wo = L_W + t * 128 + ((q ^ (t >> 1)) << 4);
      bf16x8 wf[4][4];
      auto rdw = [&](int z, int i) {
        const char* pw = smem + ((wo ^ ((i & 1) << 6)) + wpos(((i >> 1) + 2 * j - 4 * pl) & 15) * 8192);
#pragma unroll
        for (int eb = 0; eb < 4; ++eb) wf[z][eb] = *(const bf16x8*)(pw + eb * 2048);
      };
      auto grp = [&](int i0, bool more) {
        const unsigned nb0 = pb + (((i0 >> 2) + 2 + j) & 7) * 4096, nb1 = pb + (((i0 >> 2) + 3 + j) & 7) * 4096;
#pragma unroll
        for (int i = 0; i < 8; ++i) {
          if (more || i < 6) rdw((i + 2) & 3, i0 + i + 2);
          const bf16x8 pv = __builtin_bit_cast(bf16x8, pf[i]);
#pragma unroll
          for (int eb = 0; eb < 4; ++eb) ac[eb] = __builtin_amdgcn_mfma_f32_16x16x32_bf16(wf[i & 3][eb], pv, ac[eb], 0, 0, 0);
          if (more) pf[i] = LD128(l * 16 + (i & 3) * 1024, i < 4 ? nb0 : nb1);
#pragma unroll
          for (int z = 0; z < 4; ++z) { __builtin_amdgcn_sched_group_barrier(0x8, 1, 0); __builtin_amdgcn_sched_group_barrier(0x100, 1, 0); }
          __builtin_amdgcn_sched_group_barrier(0x20, 1, 0);
          fence();
        }
      };
      rdw(0, 0); rdw(1, 1);
      fence();
      ST(8);
#pragma unroll 1
      for (int i0 = 0; i0 < 24; i0 += 8) grp(i0, true);
      if (is_prod) __hip_atomic_fetch_add((LAS unsigned*)(smem + L_CNT + 28), 1u, __ATOMIC_RELAXED, __HIP_MEMORY_SCOPE_WORKGROUP);
      grp(24, false);
      ST(9);
      f32x4 s1 = {}, s2 = {}, s3 = {};
#pragma unroll
      for (int eb = 0; eb < 4; ++eb) { x1[eb] += ga[eb] * ac[eb]; s1 += x1[eb]; s2 += x1[eb] * x1[eb]; s3 += x1[eb] * uu[eb]; }
      const float S1 = sum_q((s1[0] + s1[1]) + (s1[2] + s1[3])), S2 = sum_q((s2[0] + s2[1]) + (s2[2] + s2[3])), S3 = sum_q((s3[0] + s3[1]) + (s3[2] + s3[3]));
      if (l < 32) ST128((u32x4{asu(q ? S3 : S1), tag, asu(q ? ucv : S2), tag}), WS_STATG + (b * 4 + w) * 512 + l * 16);
    }
    bar();
    ST(10);
#pragma unroll
    for (int eb = 0; eb < 4; ++eb) {
      const unsigned u0 = pk2(x1[eb][0], x1[eb][1]), u1 = pk2(x1[eb][2], x1[eb][3]);
      x1[eb] = f32x4{lo16(u0), hi16(u0), lo16(u1), hi16(u1)};
    }
    float mean, rs;
    unsigned key;
    {
      __builtin_amdgcn_s_sleep(DLY_S);
      float a0, a1;
      const unsigned so = WS_STATG + ((g + 64 * pl + 32 * (q >> 1)) * 4 + w) * 512 + (l & 31) * 16;
      SPIN {
        asm volatile("" ::: "memory");
        u32x4 z[4];
#pragma unroll
        for (int i = 0; i < 4; ++i) z[i] = LD128(so, i * 8 * 2048);
        a0 = (asf(z[0][0]) + asf(z[1][0])) + (asf(z[2][0]) + asf(z[3][0])); a1 = (asf(z[0][2]) + asf(z[1][2])) + (asf(z[2][2]) + asf(z[3][2]));
        if (none(TAG2(z[0]) | TAG2(z[1]) | TAG2(z[2]) | TAG2(z[3]))) break;
        __builtin_amdgcn_s_sleep(NAP_S);
      }
      ST(11);
      a0 = sum_x32(a0); a1 = sum_x32(a1);
      const auto e0 = __builtin_amdgcn_permlane16_swap(asu(a0), asu(a0), false, false);
      const auto e1 = __builtin_amdgcn_permlane16_swap(asu(a1), asu(a1), false, false);
      const float U = asf(__builtin_amdgcn_readlane(asu(a1), 16)), C = asf(__builtin_amdgcn_readlane(asu(a1), 17));
      mean = asf(e0[0]) * (1.f / 512);
      rs = __builtin_amdgcn_rsqf(fmaxf(asf(e1[0]) * (1.f / 512) - mean * mean, 0.f) + 1e-5f);
      const float rv = (asf(e0[1]) - mean * U) * rs + C;
      unsigned u = asu(rv); u = (u & 0x80000000u) ? ~u : (u | 0x80000000u);
      key = (u & ~2047u) | (2047 - (64 * p + 16 * w + t));
      unsigned k1 = key, k2 = 0;
      top2_row(k1, k2);
      if (j == 0 && l == 0) ST128((u32x4{k1, tag, k2, tag}), WS_CANDG + (p * 4 + w) * 128);
      if (tid == 0) *(unsigned*)(smem + L_CNT + 16) = 1u;
      ST(12);
      const bool cand = key == k1 || key == k2;
      unsigned o[8], hp[8];
#pragma unroll
      for (int eb = 0; eb < 4; ++eb) {
        const char* M = smem + L_MOD + (16 * eb + 4 * q) * 4;
        const f32x4 h = (x1[eb] - mean) * rs * *(const f32x4*)(M + 4 * 256) + *(const f32x4*)(M + 3 * 256);
        hp[2 * eb] = pk2(h[0], h[1]); hp[2 * eb + 1] = pk2(h[2], h[3]);
        const f32x4 ov = x1[eb] + *(const f32x4*)(M + 5 * 256) * f32x4{lo16(hp[2 * eb]), hi16(hp[2 * eb]), lo16(hp[2 * eb + 1]), hi16(hp[2 * eb + 1])};
        o[2 * eb] = pk2(ov[0], ov[1]); o[2 * eb + 1] = pk2(ov[2], ov[3]);
      }
      {
        char* R = smem + L_ROW + w * 512;
        if (cand) {
          char* Rm = R + (key == k1 ? 0 : 256) + 2 * q * 4;
#pragma unroll
          for (int eb = 0; eb < 4; ++eb) { *(u32x2*)(Rm + 32 * eb) = u32x2{hp[2 * eb], hp[2 * eb + 1]}; *(u32x2*)(Rm + 128 + 32 * eb) = u32x2{o[2 * eb], o[2 * eb + 1]}; }
        }
        wait_lgkm();
        const u32x2 z = *(const u32x2*)(R + (q & 1) * 256 + t * 8);
        const unsigned op = *(const unsigned*)(R + (l >> 5) * 256 + 128 + (l & 31) * 4);
        const u32x2 gb2 = *(const u32x2*)(smem + L_MOD + 5 * 256 + (l & 31) * 8);
        const unsigned so = WS_ROWG + (p * 4 + w) * 16384;
        if (l < 32) ST128((u32x4{z[0], tag, z[1], tag}), so + q * 8192 + j * 256 + t * 16);
        ST128((u32x4{(op & 0xffffu) | gb2[0], tag, (op >> 16) | gb2[1], tag}), so + (l >> 5) * 8192 + 2048 + j * 512 + (l & 31) * 16);
      }
      u16* orow = out + (size_t)(64 * p + 16 * w + t) * 512 + 64 * j + 4 * q;
      ST(13);
      bar();
      const u32x2 sel = *(const u32x2*)(smem + L_SEL);
      if (is_prod) __builtin_amdgcn_s_sleep(DLY_O);
      if (key != sel[0] && key != sel[1]) {
#pragma unroll
        for (int eb = 0; eb < 4; ++eb) *(u32x2*)(orow + 16 * eb) = u32x2{o[2 * eb], o[2 * eb + 1]};
      }
    }
  }
}

extern "C" void kernel_launch(void* const* d_in, const int* in_sizes, int n_in,
                              void* d_out, int out_size, void* d_ws, size_t ws_size,
                              hipStream_t stream) {
  fused<<<256, 512, 0, stream>>>((const u16*)d_in[0], (const u16*)d_in[1], (const u16*)d_in[2], (const u16*)d_in[3], (const u16*)d_in[4],
                                 (const u16*)d_in[6], (const u16*)d_in[7], (const u16*)d_in[8], (const u16*)d_in[9], (const u16*)d_in[10],
                                 (const u16*)d_in[11], (u16*)d_out, (char*)d_ws);
}
```
